```python
import torch

def _softmax(x, axis=-1):
    return torch.softmax(x, dim=axis)
def _gelu(x):
    return torch.nn.functional.gelu(x, approximate="tanh")
def _silu(x):
    return torch.nn.functional.silu(x)
def _sigmoid(x):
    return torch.sigmoid(x)
def _layernorm(x, axis=-1, eps=1e-5):
    m = x.mean(dim=axis, keepdim=True)
    v = x.var(dim=axis, keepdim=True, unbiased=False)
    return (x - m) / torch.sqrt(v + eps)
def _scatter_add(tgt, idx, src, axis=0):
    return tgt.clone().index_add_(axis, idx, src)
def _segment_sum(src, seg, n):
    out = torch.zeros((n,) + src.shape[1:], dtype=src.dtype)
    return out.index_add_(0, seg, src)
def _pad(x, pw):
    flat = tuple(v for pair in reversed(pw) for v in pair)
    return torch.nn.functional.pad(x, flat)

kernel_name = 'interleave_layers__gqa_block_cached__mixer_token_mlp__decode'

def setup_inputs():

    _w = (lambda a: (lambda t: t.to(torch.bfloat16) if t.is_floating_point() else t)(torch.as_tensor(a)))
    def _A_setup():
        B, Sq, D = 2, 1, 576
        Hq, Hkv, Sk, Dh = 32, 16, 16384, 128
        assert Hq % Hkv == 0, (Hq, Hkv)
        return {
            "x": _w((lambda *s: torch.randn(*s, dtype=torch.bfloat16) * 0.1)(B, Sq, D)),
            "k_cache": _w((lambda *s: torch.randn(*s, dtype=torch.bfloat16) * 0.8)(B, Hkv, Sk, Dh)),
            "v_cache": _w((lambda *s: torch.randn(*s, dtype=torch.bfloat16) * 3.2)(B, Hkv, Sk, Dh)),
            "wq": _w((lambda *s: torch.randn(*s, dtype=torch.bfloat16) * 0.8)(D, Hq * Dh) * (D ** -0.5)),
            "wo": _w((lambda *s: torch.randn(*s, dtype=torch.bfloat16) * 3.2)(Hq * Dh, D) * ((Hq * Dh) ** -0.5)),
        }
    _A_in = _A_setup()

    def _B_setup():
        B, S, D, F = 2, 1, 576, 4608
        return {
            "x": _w((lambda *s: torch.randn(*s, dtype=torch.bfloat16) * 0.1)(B, S, D)),
            "w1": _w((lambda *s: torch.randn(*s, dtype=torch.bfloat16) * 0.8)(S, F) * (S ** -0.5)),
            "b1": _w((lambda *s: torch.randn(*s, dtype=torch.bfloat16) * 0.8)(F) * 0.02),
            "w2": _w((lambda *s: torch.randn(*s, dtype=torch.bfloat16) * 0.4)(F, S) * (F ** -0.5)),
            "b2": _w((lambda *s: torch.randn(*s, dtype=torch.bfloat16) * 0.4)(S) * 0.02),
        }
    _B_in = _B_setup()

    return {
        "A_x": _A_in["x"],
        "A_k_cache": _A_in["k_cache"],
        "A_v_cache": _A_in["v_cache"],
        "A_wq": _A_in["wq"],
        "A_wo": _A_in["wo"],
        "B_x": _B_in["x"],
        "B_w1": _B_in["w1"],
        "B_b1": _B_in["b1"],
        "B_w2": _B_in["w2"],
        "B_b2": _B_in["b2"],
    }

def reference(A_x, A_k_cache, A_v_cache, A_wq, A_wo, B_x, B_w1, B_b1, B_w2, B_b2):
    def _A_ref(x, k_cache, v_cache, wq, wo):
        B, Sq, D = x.shape
        Hq, Dh = 32, 128
        Hkv = k_cache.shape[1]
        rep = Hq // Hkv
        q = (lambda x, *d: x.permute(*d))((x @ wq).reshape(B, Sq, Hq, Dh), 0, 2, 1, 3)
        k = (lambda x, n, axis: torch.repeat_interleave(x, n, dim=axis))(k_cache, rep, axis=1)
        v = (lambda x, n, axis: torch.repeat_interleave(x, n, dim=axis))(v_cache, rep, axis=1)
        scores = torch.einsum("bhqd,bhkd->bhqk", q, k) * (Dh ** -0.5)
        p = _softmax(scores, axis=-1)
        o = torch.einsum("bhqk,bhkd->bhqd", p, v)
        return (lambda x, *d: x.permute(*d))(o, 0, 2, 1, 3).reshape(B, Sq, Hq * Dh) @ wo

    def _B_ref(x, w1, b1, w2, b2):
        B, S, D = x.shape
        h = _layernorm(x, axis=-1)
        u = torch.einsum("bsd,sf->bdf", h, w1) + b1
        u = _gelu(u)
        y = torch.einsum("bdf,fs->bds", u, w2) + b2
        return (lambda x, *d: x.permute(*d))(y, 0, 2, 1)

    x = A_x
    x = x + _A_ref(_layernorm(x, axis=-1), A_k_cache, A_v_cache, A_wq, A_wo)
    x = x + _B_ref(_layernorm(x, axis=-1), B_w1, B_b1, B_w2, B_b2)
    x = x + _A_ref(_layernorm(x, axis=-1), A_k_cache, A_v_cache, A_wq, A_wo)
    x = x + _B_ref(_layernorm(x, axis=-1), B_w1, B_b1, B_w2, B_b2)
    return x
```

```cpp
#include <hip/hip_runtime.h>
#include <hip/hip_bf16.h>
#include <stdint.h>

#define KV_AUX 2
#define KV_AUX2 2

typedef unsigned v4u __attribute__((ext_vector_type(4)));
typedef float f32x4 __attribute__((ext_vector_type(4)));
typedef short bf16x8 __attribute__((ext_vector_type(8)));
typedef unsigned short u16;
constexpr int RSRC_W3 = 0x00020000;
constexpr int B = 2, D = 576, HQ = 32, HKV = 16, SK = 16384, DH = 128, F = 4608;
constexpr int NSPLIT = 8;

__device__ __forceinline__ float bf2f(u16 b) { return __builtin_bit_cast(float, (unsigned)b << 16); }
__device__ __forceinline__ unsigned cvtpk(float lo, float hi) {
  typedef float f32x2_ __attribute__((ext_vector_type(2)));
  typedef __bf16 bf16x2_ __attribute__((ext_vector_type(2)));
  f32x2_ v = {lo, hi};
  return __builtin_bit_cast(unsigned, __builtin_convertvector(v, bf16x2_));
}
__device__ __forceinline__ float rbf(float x) { return __builtin_bit_cast(float, cvtpk(x, x) << 16); }
__device__ __forceinline__ u16 f2bf(float x) { return (u16)(cvtpk(x, x) & 0xffffu); }
__device__ __forceinline__ f32x4 mfma(v4u a, v4u b, f32x4 c) {
  return __builtin_amdgcn_mfma_f32_16x16x32_bf16(__builtin_bit_cast(bf16x8, a), __builtin_bit_cast(bf16x8, b), c, 0, 0, 0);
}
__device__ __forceinline__ float wave_sum(float v) {
#pragma unroll
  for (int o = 32; o > 0; o >>= 1) v += __shfl_xor(v, o);
  return v;
}
__device__ __forceinline__ void ln9(float (&x)[9]) {
  float s = 0.f;
#pragma unroll
  for (int i = 0; i < 9; ++i) s += x[i];
  s = wave_sum(s);
  const float mean = s * (1.0f / D);
  float v = 0.f;
#pragma unroll
  for (int i = 0; i < 9; ++i) { const float d = x[i] - mean; v += d * d; }
  v = wave_sum(v) * (1.0f / D);
  const float mb = rbf(mean), vb = rbf(v);
  const float sd = rbf(sqrtf(rbf(vb + 1e-5f)));
#pragma unroll
  for (int i = 0; i < 9; ++i) x[i] = rbf(rbf(x[i] - mb) / sd);
}

__global__ void __launch_bounds__(256) k_q(const u16* x, const u16* wq, u16* q) {
  __shared__ float xn[2][D];
  __shared__ float red[4][2][64];
  const int tid = threadIdx.x, lane = tid & 63, w = tid >> 6;
  if (w < 2) {
    float v[9];
#pragma unroll
    for (int i = 0; i < 9; ++i) v[i] = bf2f(x[w * D + i * 64 + lane]);
    ln9(v);
#pragma unroll
    for (int i = 0; i < 9; ++i) xn[w][i * 64 + lane] = v[i];
  }
  __syncthreads();
  const int col = blockIdx.x * 64 + lane;
  float a0 = 0.f, a1 = 0.f;
  for (int k = w * 144; k < w * 144 + 144; ++k) { const float wv = bf2f(wq[(size_t)k * (HQ * DH) + col]); a0 += xn[0][k] * wv; a1 += xn[1][k] * wv; }
  red[w][0][lane] = a0; red[w][1][lane] = a1;
  __syncthreads();
  if (w < 2) q[w * (HQ * DH) + col] = f2bf(red[0][w][lane] + red[1][w][lane] + red[2][w][lane] + red[3][w][lane]);
}

template <int AUX>
__global__ void __launch_bounds__(256, 1) k_attn(const unsigned char* K, const unsigned char* V, const u16* q, float* part) {
  __shared__ float sm[4][2][2];
  __shared__ f32x4 so[4][64];
  __shared__ unsigned pad_lds[22 * 1024];
  const int tid = threadIdx.x, lane = tid & 63, w = __builtin_amdgcn_readfirstlane(tid >> 6);
  const int g = blockIdx.x, b = g >> 7, kvh = (g >> 3) & 15, sp = g & 7;
  if (q == nullptr) pad_lds[tid] = 1;
  const __amdgpu_buffer_rsrc_t rk = __builtin_amdgcn_make_buffer_rsrc((void*)K, 0, 128 << 20, RSRC_W3);
  const __amdgpu_buffer_rsrc_t rv = __builtin_amdgcn_make_buffer_rsrc((void*)V, 0, 128 << 20, RSRC_W3);
  const int i16 = lane & 15, gq = lane >> 4, h = (lane >> 3) & 1;
  const int koff = (4 * (i16 & 3) + (i16 >> 2)) * 256 + gq * 16;
  const int voff = lane * 16;
  constexpr int UPW = SK / NSPLIT / 16 / 4;
  const int base = g * (SK / NSPLIT * 256) + w * 4096;
  v4u qb[4];
#pragma unroll
  for (int s = 0; s < 4; ++s) qb[s] = *(const v4u*)(q + b * (HQ * DH) + (kvh * 2 + h) * DH + 32 * s + 8 * gq);
  unsigned msk[4];
#pragma unroll
  for (int i = 0; i < 4; ++i) msk[i] = (((lane & 7) >> 1) == i) ? ((lane & 1) ? 0xffff0000u : 0x0000ffffu) : 0u;
  constexpr float C2 = 0.08838834764831845f * 1.4426950408889634f;
  float m = -INFINITY, l = 0.f;
  f32x4 o = {0.f, 0.f, 0.f, 0.f};
  v4u buf[2][8];
#define ISSUE(u, s) do { const int so_ = base + (u) * 16384; \
    _Pragma("unroll") for (int t = 0; t < 4; ++t) buf[s][t] = __builtin_amdgcn_raw_buffer_load_b128(rk, koff + t * 64, so_, AUX); \
    _Pragma("unroll") for (int t = 0; t < 4; ++t) buf[s][4 + t] = __builtin_amdgcn_raw_buffer_load_b128(rv, voff + t * 1024, so_, AUX); \
    __builtin_amdgcn_sched_barrier(0); } while (0)
  ISSUE(0, 0); ISSUE(1, 1);
#pragma unroll 2
  for (int u = 0; u < UPW; ++u) {
    const int s = u & 1;
    f32x4 sc = {0.f, 0.f, 0.f, 0.f};
#pragma unroll
    for (int t = 0; t < 4; ++t) sc = mfma(buf[s][t], qb[t], sc);
    sc *= C2;
    float mx = fmaxf(fmaxf(sc[0], sc[1]), fmaxf(sc[2], sc[3]));
    if (__any(mx > m)) {
      mx = fmaxf(mx, __shfl_xor(mx, 16)); mx = fmaxf(mx, __shfl_xor(mx, 32));
      const float mn = fmaxf(m, mx);
      const float al = __builtin_amdgcn_exp2f(m - mn);
      o *= al; l *= al; m = mn;
    }
#pragma unroll
    for (int t = 0; t < 4; ++t) {
      const float p = __builtin_amdgcn_exp2f(sc[t] - m);
      l += p;
      const unsigned pp = cvtpk(p, p);
      v4u bb = {pp & msk[0], pp & msk[1], pp & msk[2], pp & msk[3]};
      o = mfma(buf[s][4 + t], bb, o);
    }
    __builtin_amdgcn_sched_barrier(0);
    if (u + 2 < UPW) ISSUE(u + 2, s);
  }
  l += __shfl_xor(l, 16); l += __shfl_xor(l, 32);
  if ((lane & 7) == 0 && lane < 16) { sm[w][h][0] = m; sm[w][h][1] = l; }
  so[w][lane] = o;
  __syncthreads();
  if (w == 0) {
    float M = fmaxf(fmaxf(sm[0][h][0], sm[1][h][0]), fmaxf(sm[2][h][0], sm[3][h][0]));
    float L = 0.f; f32x4 O = {0.f, 0.f, 0.f, 0.f};
#pragma unroll
    for (int ww = 0; ww < 4; ++ww) { const float a = __builtin_amdgcn_exp2f(sm[ww][h][0] - M); L += a * sm[ww][h][1]; O += a * so[ww][lane]; }
    float* p = part + ((size_t)(b * HQ + kvh * 2 + h) * NSPLIT + sp) * 132;
#pragma unroll
    for (int r = 0; r < 4; ++r) p[8 * (4 * gq + r) + (lane & 7)] = O[r];
    if ((lane & 7) == 0 && lane < 16) { p[128] = M; p[129] = L; }
  }
}

__global__ void __launch_bounds__(256) k_wo(const float* part, const u16* wo, const u16* xin, u16* xout) {
  __shared__ float o[HQ * DH];
  __shared__ float red[16][16];
  const int tid = threadIdx.x, b = blockIdx.x / 36, cg = blockIdx.x % 36;
  for (int i = tid; i < HQ * DH; i += 256) {
    const int hh = i >> 7, d = i & 127;
    const float* p = part + (size_t)(b * HQ + hh) * NSPLIT * 132;
    float M = -INFINITY;
#pragma unroll
    for (int s = 0; s < NSPLIT; ++s) M = fmaxf(M, p[s * 132 + 128]);
    float L = 0.f, O = 0.f;
#pragma unroll
    for (int s = 0; s < NSPLIT; ++s) { const float a = __builtin_amdgcn_exp2f(p[s * 132 + 128] - M); L += a * p[s * 132 + 129]; O += a * p[s * 132 + d]; }
    o[i] = rbf(O / L);
  }
  __syncthreads();
  const int c = tid & 15, kg = tid >> 4;
  float a = 0.f;
  for (int k = kg; k < HQ * DH; k += 16) a += o[k] * bf2f(wo[(size_t)k * D + cg * 16 + c]);
  red[kg][c] = a;
  __syncthreads();
  if (tid < 16) {
    float s = 0.f;
#pragma unroll
    for (int i = 0; i < 16; ++i) s += red[i][tid];
    xout[b * D + cg * 16 + tid] = f2bf(bf2f(xin[b * D + cg * 16 + tid]) + rbf(s));
  }
}

__device__ __forceinline__ float gelu_tanh(float u) {
  const float z2 = u * (1.0f + 0.044715f * u * u) * (2.0f * 0.7978845608028654f * 1.4426950408889634f);
  const float e = __builtin_amdgcn_exp2f(-z2);
  return u * __builtin_amdgcn_rcpf(1.0f + e);
}

__global__ void __launch_bounds__(256) k_mlp(const u16* xin, const u16* w1, const u16* b1, const u16* w2, const u16* b2, u16* xout) {
  const int tid = threadIdx.x, lane = tid & 63, w = tid >> 6;
  const int idx = blockIdx.x * 4 + w, b = idx / D, d = idx % D;
  float v[9];
#pragma unroll
  for (int i = 0; i < 9; ++i) v[i] = bf2f(xin[b * D + i * 64 + lane]);
  ln9(v); ln9(v);
  float hsel = 0.f;
#pragma unroll
  for (int i = 0; i < 9; ++i) if (i == (d >> 6)) hsel = v[i];
  const float h = __shfl(hsel, d & 63);
  float a = 0.f;
  for (int f = lane; f < F; f += 64) a += gelu_tanh(h * bf2f(w1[f]) + bf2f(b1[f])) * bf2f(w2[f]);
  a = wave_sum(a);
  if (lane == 0) xout[idx] = f2bf(bf2f(xin[idx]) + rbf(rbf(a) + bf2f(b2[0])));
}

extern "C" void kernel_launch(void* const* d_in, const int* in_sizes, int n_in, void* d_out, int out_size, void* d_ws, size_t ws_size, hipStream_t stream) {
  const u16* x = (const u16*)d_in[0];
  const unsigned char* K = (const unsigned char*)d_in[1];
  const unsigned char* V = (const unsigned char*)d_in[2];
  const u16* wq = (const u16*)d_in[3];
  const u16* wo = (const u16*)d_in[4];
  const u16* w1 = (const u16*)d_in[6];
  const u16* b1 = (const u16*)d_in[7];
  const u16* w2 = (const u16*)d_in[8];
  const u16* b2 = (const u16*)d_in[9];
  unsigned char* ws = (unsigned char*)d_ws;
  u16* q = (u16*)ws;
  u16* x1 = (u16*)(ws + 32768);
  u16* x2 = (u16*)(ws + 32768 + 4096);
  u16* x3 = (u16*)(ws + 32768 + 8192);
  float* part = (float*)(ws + 65536);
  k_q<<<64, 256, 0, stream>>>(x, wq, q);
  k_attn<KV_AUX><<<256, 256, 0, stream>>>(K, V, q, part);
  k_wo<<<72, 256, 0, stream>>>(part, wo, x, x1);
  k_mlp<<<288, 256, 0, stream>>>(x1, w1, b1, w2, b2, x2);
  k_q<<<64, 256, 0, stream>>>(x2, wq, q);
  k_attn<KV_AUX2><<<256, 256, 0, stream>>>(K, V, q, part);
  k_wo<<<72, 256, 0, stream>>>(part, wo, x2, x3);
  k_mlp<<<288, 256, 0, stream>>>(x3, w1, b1, w2, b2, (u16*)d_out);
}
```

```cpp
#include <hip/hip_runtime.h>

__global__ void __launch_bounds__(256, 1)
fused_decode(const void* x, const void* kc, const void* vc, const void* wq, const void* wo, const void* w1, const void* b1, const void* w2, const void* b2, void* out, void* ws) {
  unsigned tid = threadIdx.x;
  asm volatile(
    "s_load_dwordx16 s[44:59], %0, 0\n"
    "s_load_dwordx4 s[60:63], %0, 64\n"
    "s_load_dwordx2 s[16:17], %0, 80\n"
    "s_mov_b32 s29, %1\n"
    "v_lshrrev_b32 v8, 6, %2\n"
    "v_mbcnt_lo_u32_b32 v9, -1, 0\n"
    "v_readfirstlane_b32 s28, v8\n"
    "v_mbcnt_hi_u32_b32 v9, -1, v9\n"
    "s_getpc_b64 s[24:25]\n"
    ".Lcode:\n"
    "s_and_b32 s25, s25, 0xffff\n"
    "s_mov_b32 s26, .Lend-.Lcode\n"
    "s_mov_b32 s27, 0x20000\n"
    "v_lshlrev_b32 v10, 6, %2\n"
    "v_add_u32 v11, 0x4000, v10\n"
    "buffer_load_dword v254, v10, s[24:27], 0 offen\n"
    "buffer_load_dword v255, v11, s[24:27], 0 offen\n"
    "s_getpc_b64 s[34:35]\n"
    ".Lpc17_.Lstream:\n"
    "s_add_u32 s34, s34, .Lstream-.Lpc17_.Lstream\n"
    "s_addc_u32 s35, s35, 0\n"
    "v_lshlrev_b32 v4, 4, v9\n"
    "v_lshlrev_b32 v10, 9, v9\n"
    "v_and_b32 v10, 3072, v10\n"
    "v_and_b32 v11, 1, v9\n"
    "v_lshl_or_b32 v10, v11, 8, v10\n"
    "v_lshrrev_b32 v11, 3, v9\n"
    "v_xor_b32 v1, v9, v11\n"
    "v_and_b32 v1, 7, v1\n"
    "v_and_b32 v13, 1, v11\n"
    "v_lshl_or_b32 v1, v13, 4, v1\n"
    "v_lshrrev_b32 v13, 1, v11\n"
    "v_lshl_or_b32 v1, v13, 6, v1\n"
    "v_lshlrev_b32 v1, 4, v1\n"
    "s_and_b32 s64, s29, 7\n"
    "s_lshr_b32 s65, s29, 3\n"
    "s_lshl_b32 s66, s64, 5\n"
    "s_add_u32 s66, s66, s65\n"
    "s_lshl_b32 s66, s66, 18\n"
    "s_lshl_b32 s73, s28, 12\n"
    "s_lshl_b32 s67, s28, 14\n"
    "s_add_u32 s32, s66, s67\n"
    "s_mov_b32 s33, s73\n"
    "s_and_b32 s31, s65, 15\n"
    "s_lshr_b32 s65, s29, 7\n"
    "s_lshl_b32 s64, s64, 1\n"
    "s_add_u32 s30, s64, s65\n"
    "s_waitcnt lgkmcnt(0)\n"
    "s_mov_b32 s8, s46\n"
    "s_and_b32 s9, s47, 0xffff\n"
    "s_mov_b32 s10, 134217728\n"
    "s_mov_b32 s11, 0x20000\n"
    "s_swappc_b64 s[36:37], s[34:35]\n"
    "v_and_b32 v12, 1, v11\n"
    "v_lshl_or_b32 v2, v12, 6, v10\n"
    "v_and_b32 v12, 48, v9\n"
    "v_or_b32 v2, v2, v12\n"
    "v_and_b32 v13, 15, v9\n"
    "v_lshlrev_b32 v3, 3, v13\n"
    "v_mul_u32_u24 v12, 0xa0, v12\n"
    "v_and_b32 v12, 0x1200, v12\n"
    "v_or_b32 v3, v3, v12\n"
    "v_and_b32 v12, 3, v9\n"
    "v_mul_u32_u24 v5, 0x5555, v12\n"
    "s_and_b32 s17, s17, 0xffff\n"
    "s_mov_b32 s18, 1839104\n"
    "s_mov_b32 s19, 0x20000\n"
    "s_mov_b32 s24, s60\n"
    "s_and_b32 s25, s61, 0xffff\n"
    "s_mov_b32 s26, 2\n"
    "s_mov_b32 s27, 0x20000\n"
    "buffer_load_dwordx2 v[14:15], off, s[16:19], 0 sc1\n"
    "buffer_load_ushort v13, off, s[24:27], 0\n"
    "s_mov_b32 s24, s50\n"
    "s_and_b32 s25, s51, 0xffff\n"
    "s_mov_b32 s26, 4718592\n"
    "s_mov_b32 s27, 0x20000\n"
    "v_and_b32 v12, 0x70, v4\n"
    "v_lshl_or_b32 v12, v11, 13, v12\n"
    "s_lshr_b32 s64, s31, 2\n"
    "s_and_b32 s65, s31, 3\n"
    "s_lshl_b32 s65, s65, 7\n"
    "s_lshl_b32 s66, s30, 9\n"
    "s_add_u32 s65, s65, s66\n"
    "s_lshl_b32 s66, s64, 20\n"
    "s_add_u32 s66, s66, s65\n"
    "s_lshl_b32 s67, s28, 16\n"
    "s_add_u32 s66, s66, s67\n"
    "s_lshl_b32 s67, s28, 10\n"
    "s_add_u32 m0, s67, 76800\n"
    "s_nop 0\n"
    "buffer_load_dwordx4 v12, s[24:27], s66 offen nt lds\n"
    "s_add_u32 m0, m0, 4096\n"
    "s_add_u32 s66, s66, 0x40000\n"
    "buffer_load_dwordx4 v12, s[24:27], s66 offen nt lds\n"
    "s_add_u32 m0, m0, 4096\n"
    "s_add_u32 s66, s66, 0x40000\n"
    "buffer_load_dwordx4 v12, s[24:27], s66 offen nt lds\n"
    "s_add_u32 m0, m0, 4096\n"
    "s_add_u32 s66, s66, 0x40000\n"
    "buffer_load_dwordx4 v12, s[24:27], s66 offen nt lds\n"
    "s_cmp_lg_u32 s28, 0\n"
    "s_cbranch_scc1 .Lp_w\n"
    "s_lshl_b32 s66, s64, 17\n"
    "s_add_u32 s66, s66, s65\n"
    "s_add_u32 s66, s66, 4194304\n"
    "s_mov_b32 m0, 93184\n"
    "v_mov_b32 v16, -1\n"
    "buffer_load_dwordx4 v12, s[24:27], s66 offen nt lds\n"
    "s_add_u32 m0, m0, 1024\n"
    "s_add_u32 s66, s66, 0x10000\n"
    "buffer_load_dwordx4 v12, s[24:27], s66 offen nt lds\n"
    "s_mov_b32 s24, s44\n"
    "s_and_b32 s25, s45, 0xffff\n"
    "s_mov_b32 s26, 2304\n"
    "s_mov_b32 s27, 0x20000\n"
    "s_mov_b32 m0, 95232\n"
    "v_mov_b32 v17, -1\n"
    "buffer_load_dwordx4 v4, s[24:27], 0 offen offset:0 lds\n"
    "buffer_load_dwordx4 v4, s[24:27], 0 offen offset:1024 lds\n"
    "buffer_load_dwordx4 v4, s[24:27], 0 offen offset:2048 lds\n"
    "v_mov_b32 v18, -1\n"
    "v_mov_b32 v19, -1\n"
    "s_mul_i32 s64, s29, 7168\n"
    "s_add_u32 s64, s64, 4096\n"
    "s_add_u32 s65, s64, 4096\n"
    "s_add_u32 s66, s64, 8192\n"
    "buffer_store_dwordx4 v[16:19], v4, s[16:19], s64 offen offset:0 sc1\n"
    "buffer_store_dwordx4 v[16:19], v4, s[16:19], s64 offen offset:1024 sc1\n"
    "buffer_store_dwordx4 v[16:19], v4, s[16:19], s64 offen offset:2048 sc1\n"
    "buffer_store_dwordx4 v[16:19], v4, s[16:19], s64 offen offset:3072 sc1\n"
    "buffer_store_dwordx4 v[16:19], v4, s[16:19], s65 offen offset:0 sc1\n"
    "buffer_store_dwordx4 v[16:19], v4, s[16:19], s65 offen offset:1024 sc1\n"
    "buffer_store_dwordx4 v[16:19], v4, s[16:19], s65 offen offset:2048 sc1\n"
    "v_mov_b32 v20, 0\n"
    "v_mov_b32 v21, 0\n"
    "v_and_b32 v22, 24, v3\n"
    "v_add_u32 v22, 98048, v22\n"
    "ds_write_b64 v22, v[20:21]\n"
    "s_branch .Lp_j\n"
    ".Lp_w:\n"
    "s_cmp_eq_u32 s28, 2\n"
    "s_cselect_b32 s24, s56, s58\n"
    "s_cselect_b32 s25, s57, s59\n"
    "s_cmp_eq_u32 s28, 1\n"
    "s_cselect_b32 s24, s54, s24\n"
    "s_cselect_b32 s25, s55, s25\n"
    "s_and_b32 s25, s25, 0xffff\n"
    "s_mov_b32 s26, 9216\n"
    "s_mov_b32 s27, 0x20000\n"
    "s_mul_i32 s64, s28, 9216\n"
    "s_add_u32 m0, s64, 89088\n"
    "s_mov_b32 s64, 4096\n"
    "s_mov_b32 s65, 8192\n"
    "buffer_load_dwordx4 v4, s[24:27], 0 offen offset:0 lds\n"
    "buffer_load_dwordx4 v4, s[24:27], 0 offen offset:1024 lds\n"
    "buffer_load_dwordx4 v4, s[24:27], 0 offen offset:2048 lds\n"
    "buffer_load_dwordx4 v4, s[24:27], 0 offen offset:3072 lds\n"
    "s_add_u32 m0, m0, 4096\n"
    "s_nop 0\n"
    "buffer_load_dwordx4 v4, s[24:27], s64 offen offset:0 lds\n"
    "buffer_load_dwordx4 v4, s[24:27], s64 offen offset:1024 lds\n"
    "buffer_load_dwordx4 v4, s[24:27], s64 offen offset:2048 lds\n"
    "buffer_load_dwordx4 v4, s[24:27], s64 offen offset:3072 lds\n"
    "s_add_u32 m0, m0, 4096\n"
    "s_nop 0\n"
    "buffer_load_dwordx4 v4, s[24:27], s65 offen offset:0 lds\n"
    ".Lp_j:\n"
    "s_swappc_b64 s[36:37], s[34:35]\n"
    "s_swappc_b64 s[36:37], s[34:35]\n"
    "s_mov_b32 s12, s48\n"
    "s_and_b32 s13, s49, 0xffff\n"
    "s_mov_b32 s14, 134217728\n"
    "s_mov_b32 s15, 0x20000\n"
    "s_mov_b32 s20, s52\n"
    "s_and_b32 s21, s53, 0xffff\n"
    "s_mov_b32 s22, 4718592\n"
    "s_mov_b32 s23, 0x20000\n"
    "s_mov_b32 s74, 0x3f800000\n"
    "s_mov_b32 s75, 0x40000000\n"
    "s_mov_b32 s76, 0x40800000\n"
    "s_mov_b32 s77, 0x41000000\n"
    "s_mov_b32 s78, 0x41800000\n"
    "s_mov_b32 s79, 0x42000000\n"
    "s_mov_b32 s80, 0x42800000\n"
    "s_mov_b32 s81, 0x43000000\n"
    "s_mov_b32 s82, 0x43800000\n"
    "s_mov_b32 s83, 0x44000000\n"
    "s_mov_b32 s84, 0x44800000\n"
    "s_mov_b32 s85, 0x45000000\n"
    "s_mov_b32 s86, 0x45800000\n"
    "s_mov_b32 s87, 0x46000000\n"
    "s_mov_b32 s88, 0x46800000\n"
    "s_mov_b32 s89, 0x47000000\n"
    "s_mov_b32 s90, 0x47800000\n"
    "s_mov_b32 s91, 0x48000000\n"
    "s_mov_b32 s92, 0x48800000\n"
    "s_mov_b32 s93, 0x49000000\n"
    "s_mov_b32 s94, 0x49800000\n"
    "s_mov_b32 s95, 0x4a000000\n"
    "s_and_b32 s46, s32, 67108863\n"
    "s_mov_b32 s40, 400000\n"
    "s_mov_b32 s41, 0\n"
    "s_mov_b32 s98, 0x3ae38e39\n"
    "s_mov_b32 s42, 147456\n"
    "s_and_b32 s64, s29, 127\n"
    "s_lshl_b32 s64, s64, 16\n"
    "s_lshr_b32 s65, s29, 7\n"
    "s_lshl_b32 s65, s65, 31\n"
    "s_or_b32 s64, s64, s65\n"
    "s_or_b32 s64, s64, 922746880\n"
    "v_mov_b32 v16, 0x3eeff558\n"
    "v_mul_f32 v16, s64, v16\n"
    "s_nop 0\n"
    "v_readfirstlane_b32 s99, v16\n"
    "s_getpc_b64 s[96:97]\n"
    ".Lpc215_.Llnq0:\n"
    "s_add_u32 s96, s96, .Llnq0-.Lpc215_.Llnq0\n"
    "s_addc_u32 s97, s97, 0\n"
    "s_waitcnt vmcnt(8) lgkmcnt(0)\n"
    "s_barrier\n"
    "v_readfirstlane_b32 s64, v14\n"
    "v_readfirstlane_b32 s65, v15\n"
    "v_readfirstlane_b32 s43, v13\n"
    "s_add_u32 s64, s64, 0x7f4a7c15\n"
    "s_addc_u32 s65, s65, 0x9e3779b9\n"
    "s_lshl_b32 s43, s43, 16\n"
    "s_lshr_b64 s[66:67], s[64:65], 30\n"
    "s_xor_b64 s[64:65], s[64:65], s[66:67]\n"
    "s_mul_i32 s66, s64, 0xbf58476d\n"
    "s_mul_hi_u32 s67, s64, 0x1ce4e5b9\n"
    "s_add_u32 s66, s66, s67\n"
    "s_mul_i32 s65, s65, 0x1ce4e5b9\n"
    "s_add_u32 s65, s65, s66\n"
    "s_mul_i32 s64, s64, 0x1ce4e5b9\n"
    "s_lshr_b64 s[66:67], s[64:65], 27\n"
    "s_xor_b64 s[64:65], s[64:65], s[66:67]\n"
    "s_mul_i32 s66, s64, 0x94d049bb\n"
    "s_mul_hi_u32 s67, s64, 0x133111eb\n"
    "s_add_u32 s66, s66, s67\n"
    "s_mul_i32 s65, s65, 0x133111eb\n"
    "s_add_u32 s65, s65, s66\n"
    "s_mul_i32 s64, s64, 0x133111eb\n"
    "s_lshr_b64 s[66:67], s[64:65], 31\n"
    "s_xor_b64 s[38:39], s[64:65], s[66:67]\n"
    "s_cmp_lg_u32 s28, 0\n"
    "s_cbranch_scc1 .Lp_r\n"
    "v_mov_b32 v16, s38\n"
    "v_mov_b32 v17, s39\n"
    "s_lshl_b32 s64, s29, 3\n"
    "buffer_store_dwordx2 v[16:17], off, s[16:19], s64 offset:256 sc1\n"
    ".Lp_r:\n"
    "s_mov_b32 s51, 0\n"
    "s_swappc_b64 s[44:45], s[96:97]\n"
    "s_swappc_b64 s[36:37], s[34:35]\n"
    "s_mov_b32 s51, 1\n"
    "s_swappc_b64 s[44:45], s[96:97]\n"
    "s_swappc_b64 s[36:37], s[34:35]\n"
    "s_getpc_b64 s[96:97]\n"
    ".Lpc258_.Ltab:\n"
    "s_add_u32 s96, s96, .Ltab-.Lpc258_.Ltab\n"
    "s_addc_u32 s97, s97, 0\n"
    "v_mov_b32 v16, 0\n"
    "v_mov_b32 v17, 0\n"
    "v_mov_b32 v18, 0\n"
    "v_mov_b32 v19, 0\n"
    "v_mov_b32 v20, 0\n"
    "v_mov_b32 v21, 0\n"
    "v_mov_b32 v22, 0\n"
    "v_mov_b32 v23, 0\n"
    "v_mov_b32 v24, 0\n"
    "v_mov_b32 v25, 0\n"
    "v_mov_b32 v26, 0\n"
    "v_mov_b32 v27, 0\n"
    "v_mov_b32 v28, 0\n"
    "v_mov_b32 v29, 0\n"
    "v_mov_b32 v30, 0\n"
    "v_mov_b32 v31, 0\n"
    "v_mov_b32 v32, 0\n"
    "v_mov_b32 v33, 0\n"
    "v_mov_b32 v34, 0\n"
    "v_mov_b32 v35, 0\n"
    "v_mov_b32 v36, 0\n"
    "v_mov_b32 v37, 0\n"
    "v_mov_b32 v38, 0\n"
    "v_mov_b32 v39, 0\n"
    "v_mov_b32 v40, 0\n"
    "v_lshrrev_b32 v8, 3, v4\n"
    "s_lshl_b32 s64, s28, 7\n"
    "s_add_u32 s64, s64, 98304\n"
    "v_add_u32 v8, s64, v8\n"
    "s_mov_b32 s52, 0x409d30fc\n"
    "s_mov_b32 s53, 0x7e000000\n"
    "ds_read_u16_d16_hi v38, v8\n"
    "ds_read_u16_d16_hi v39, v8 offset:9216\n"
    "ds_read_u16_d16_hi v40, v8 offset:18432\n"
    "s_swappc_b64 s[44:45], s[96:97]\n"
    "s_mov_b64 s[44:45], s[34:35]\n"
    "s_swappc_b64 s[36:37], s[96:97]\n"
    "s_mov_b64 s[44:45], s[34:35]\n"
    "s_swappc_b64 s[36:37], s[96:97]\n"
    "s_swappc_b64 s[44:45], s[96:97]\n"
    "s_mov_b64 s[44:45], s[34:35]\n"
    "s_swappc_b64 s[36:37], s[96:97]\n"
    "buffer_load_dwordx4 a[240:243], v4, s[16:19], 0 offen offset:256 sc1\n"
    "buffer_load_dwordx4 a[244:247], v4, s[16:19], 0 offen offset:1280 sc1\n"
    "s_mov_b64 s[44:45], s[34:35]\n"
    "s_swappc_b64 s[36:37], s[96:97]\n"
    "s_swappc_b64 s[44:45], s[96:97]\n"
    "s_mov_b64 s[44:45], s[34:35]\n"
    "s_swappc_b64 s[36:37], s[96:97]\n"
    "s_mov_b64 s[44:45], s[34:35]\n"
    "s_swappc_b64 s[36:37], s[96:97]\n"
    "s_swappc_b64 s[44:45], s[96:97]\n"
    "s_mov_b64 s[44:45], s[34:35]\n"
    "s_swappc_b64 s[36:37], s[96:97]\n"
    "s_mov_b64 s[44:45], s[34:35]\n"
    "s_swappc_b64 s[36:37], s[96:97]\n"
    "s_waitcnt vmcnt(20)\n"
    ".Lrdy_chk:\n"
    "v_accvgpr_read_b32 v44, a240\n"
    "v_accvgpr_read_b32 v45, a241\n"
    "v_accvgpr_read_b32 v46, a242\n"
    "v_accvgpr_read_b32 v47, a243\n"
    "v_accvgpr_read_b32 v48, a244\n"
    "v_accvgpr_read_b32 v49, a245\n"
    "v_accvgpr_read_b32 v50, a246\n"
    "v_accvgpr_read_b32 v51, a247\n"
    "v_xor_b32 v44, s38, v44\n"
    "v_xor_b32 v45, s39, v45\n"
    "v_xor_b32 v46, s38, v46\n"
    "v_xor_b32 v47, s39, v47\n"
    "v_xor_b32 v48, s38, v48\n"
    "v_xor_b32 v49, s39, v49\n"
    "v_xor_b32 v50, s38, v50\n"
    "v_xor_b32 v51, s39, v51\n"
    "v_or3_b32 v44, v44, v45, v46\n"
    "v_or3_b32 v47, v47, v48, v49\n"
    "v_or3_b32 v44, v44, v50, v51\n"
    "v_or_b32 v44, v44, v47\n"
    "v_cmp_ne_u32 vcc, 0, v44\n"
    "s_cbranch_vccnz .Lrdy_re\n"
    ".Lrdy_ok:\n"
    "s_barrier\n"
    "s_lshl_b32 s50, s28, 17\n"
    "s_lshl_b32 s64, s30, 12\n"
    "s_add_u32 s50, s50, s64\n"
    "s_add_u32 s50, s50, 1183744\n"
    "s_cmp_gt_u32 s28, 1\n"
    "s_cbranch_scc1 .Lrdy_e\n"
    "v_lshrrev_b32 v48, 2, v4\n"
    "s_add_u32 s64, s73, 150528\n"
    "v_add_u32 v49, s64, v48\n"
    "s_lshl_b32 s64, s31, 8\n"
    "s_add_u32 s64, s64, s50\n"
    "ds_read2st64_b32 v[44:45], v49 offset1:1\n"
    "ds_read2st64_b32 v[46:47], v49 offset0:2 offset1:3\n"
    "s_waitcnt lgkmcnt(0)\n"
    "v_pk_add_f32 v[44:45], v[44:45], v[46:47]\n"
    "v_add_f32 v44, v44, v45\n"
    "v_min_u32 v44, -2, v44\n"
    "buffer_store_dword v44, v48, s[16:19], s64 offen sc1\n"
    "s_branch .Lrdy_x\n"
    ".Lrdy_e:\n"
    "s_add_u32 s64, s29, s28\n"
    "s_cmp_lg_u32 s64, 2\n"
    "s_cbranch_scc1 .Lrdy_x\n"
    "v_mov_b32 v44, s38\n"
    "v_mov_b32 v45, s39\n"
    "buffer_store_dwordx2 v[44:45], off, s[16:19], 0 sc1\n"
    ".Lrdy_x:\n"
    "s_swappc_b64 s[44:45], s[96:97]\n"
    "s_mov_b64 s[44:45], s[34:35]\n"
    "s_swappc_b64 s[36:37], s[96:97]\n"
    "s_mov_b64 s[44:45], s[34:35]\n"
    "s_swappc_b64 s[36:37], s[96:97]\n"
    "s_swappc_b64 s[44:45], s[96:97]\n"
    "s_mov_b64 s[44:45], s[34:35]\n"
    "s_swappc_b64 s[36:37], s[96:97]\n"
    "s_mov_b64 s[44:45], s[34:35]\n"
    "s_swappc_b64 s[36:37], s[96:97]\n"
    "s_cmp_gt_u32 s28, 1\n"
    "s_cbranch_scc1 .Lq0_i\n"
    "buffer_load_dwordx4 a[240:243], v4, s[16:19], s50 offen offset:0 sc1\n"
    "buffer_load_dwordx4 a[244:247], v4, s[16:19], s50 offen offset:1024 sc1\n"
    "buffer_load_dwordx4 a[248:251], v4, s[16:19], s50 offen offset:2048 sc1\n"
    "buffer_load_dwordx4 a[252:255], v4, s[16:19], s50 offen offset:3072 sc1\n"
    ".Lq0_i:\n"
    "s_waitcnt lgkmcnt(0)\n"
    "v_mov_b32 v38, v36\n"
    "v_mov_b32 v39, v37\n"
    "v_permlane32_swap_b32 v16, v18\n"
    "v_permlane32_swap_b32 v17, v19\n"
    "v_permlane32_swap_b32 v20, v22\n"
    "v_permlane32_swap_b32 v21, v23\n"
    "v_permlane32_swap_b32 v24, v26\n"
    "v_permlane32_swap_b32 v25, v27\n"
    "v_permlane32_swap_b32 v28, v30\n"
    "v_permlane32_swap_b32 v29, v31\n"
    "v_permlane32_swap_b32 v32, v34\n"
    "v_permlane32_swap_b32 v33, v35\n"
    "v_permlane32_swap_b32 v36, v38\n"
    "v_permlane32_swap_b32 v37, v39\n"
    "v_pk_add_f32 v[16:17], v[16:17], v[18:19]\n"
    "v_pk_add_f32 v[20:21], v[20:21], v[22:23]\n"
    "v_pk_add_f32 v[24:25], v[24:25], v[26:27]\n"
    "v_pk_add_f32 v[28:29], v[28:29], v[30:31]\n"
    "v_pk_add_f32 v[32:33], v[32:33], v[34:35]\n"
    "v_pk_add_f32 v[36:37], v[36:37], v[38:39]\n"
    "v_permlane16_swap_b32 v16, v17\n"
    "v_permlane16_swap_b32 v20, v21\n"
    "v_permlane16_swap_b32 v24, v25\n"
    "v_permlane16_swap_b32 v28, v29\n"
    "v_permlane16_swap_b32 v32, v33\n"
    "v_permlane16_swap_b32 v36, v37\n"
    "v_add_f32 v16, v16, v17\n"
    "v_add_f32 v20, v20, v21\n"
    "v_add_f32 v24, v24, v25\n"
    "v_add_f32 v28, v28, v29\n"
    "v_add_f32 v32, v32, v33\n"
    "v_add_f32 v36, v36, v37\n"
    "v_add_f32_dpp v16, v16, v16 quad_perm:[1,0,3,2] row_mask:0xf bank_mask:0xf\n"
    "v_add_f32_dpp v20, v20, v20 quad_perm:[1,0,3,2] row_mask:0xf bank_mask:0xf\n"
    "v_add_f32_dpp v24, v24, v24 quad_perm:[1,0,3,2] row_mask:0xf bank_mask:0xf\n"
    "v_add_f32_dpp v28, v28, v28 quad_perm:[1,0,3,2] row_mask:0xf bank_mask:0xf\n"
    "v_add_f32_dpp v32, v32, v32 quad_perm:[1,0,3,2] row_mask:0xf bank_mask:0xf\n"
    "v_add_f32_dpp v36, v36, v36 quad_perm:[1,0,3,2] row_mask:0xf bank_mask:0xf\n"
    "v_add_f32_dpp v16, v16, v16 quad_perm:[2,3,0,1] row_mask:0xf bank_mask:0xf\n"
    "v_add_f32_dpp v20, v20, v20 quad_perm:[2,3,0,1] row_mask:0xf bank_mask:0xf\n"
    "v_add_f32_dpp v24, v24, v24 quad_perm:[2,3,0,1] row_mask:0xf bank_mask:0xf\n"
    "v_add_f32_dpp v28, v28, v28 quad_perm:[2,3,0,1] row_mask:0xf bank_mask:0xf\n"
    "v_add_f32_dpp v32, v32, v32 quad_perm:[2,3,0,1] row_mask:0xf bank_mask:0xf\n"
    "v_add_f32_dpp v36, v36, v36 quad_perm:[2,3,0,1] row_mask:0xf bank_mask:0xf\n"
    "v_add_f32_dpp v16, v16, v16 row_half_mirror row_mask:0xf bank_mask:0xf\n"
    "v_add_f32_dpp v20, v20, v20 row_half_mirror row_mask:0xf bank_mask:0xf\n"
    "v_add_f32_dpp v24, v24, v24 row_half_mirror row_mask:0xf bank_mask:0xf\n"
    "v_add_f32_dpp v28, v28, v28 row_half_mirror row_mask:0xf bank_mask:0xf\n"
    "v_add_f32_dpp v32, v32, v32 row_half_mirror row_mask:0xf bank_mask:0xf\n"
    "v_add_f32_dpp v36, v36, v36 row_half_mirror row_mask:0xf bank_mask:0xf\n"
    "v_add_f32_dpp v16, v16, v16 row_mirror row_mask:0xf bank_mask:0xf\n"
    "v_add_f32_dpp v20, v20, v20 row_mirror row_mask:0xf bank_mask:0xf\n"
    "v_add_f32_dpp v24, v24, v24 row_mirror row_mask:0xf bank_mask:0xf\n"
    "v_add_f32_dpp v28, v28, v28 row_mirror row_mask:0xf bank_mask:0xf\n"
    "v_add_f32_dpp v32, v32, v32 row_mirror row_mask:0xf bank_mask:0xf\n"
    "v_add_f32_dpp v36, v36, v36 row_mirror row_mask:0xf bank_mask:0xf\n"
    "v_lshrrev_b32 v8, 8, v4\n"
    "s_lshl_b32 s64, s28, 7\n"
    "s_add_u32 s64, s64, 162816\n"
    "v_lshl_add_u32 v8, v8, 2, s64\n"
    "ds_write2_b32 v8, v16, v20 offset0:0 offset1:4\n"
    "ds_write2_b32 v8, v24, v28 offset0:8 offset1:12\n"
    "ds_write2_b32 v8, v32, v36 offset0:16 offset1:20\n"
    "s_waitcnt lgkmcnt(0)\n"
    "s_barrier\n"
    "s_cmp_lg_u32 s28, 0\n"
    "s_cbranch_scc1 .Ltab_x\n"
    "v_lshrrev_b32 v9, 1, v4\n"
    "v_and_b32 v9, 0x78, v9\n"
    "v_add_u32 v8, 162816, v9\n"
    "ds_read2_b64 v[16:19], v8 offset1:16\n"
    "ds_read2_b64 v[20:23], v8 offset0:32 offset1:48\n"
    "v_lshrrev_b32 v9, 1, v9\n"
    "s_lshl_b32 s64, s29, 6\n"
    "s_add_u32 s64, s64, 1445888\n"
    "s_mov_b64 exec, 0xfff\n"
    "s_waitcnt lgkmcnt(0)\n"
    "v_pk_add_f32 v[16:17], v[16:17], v[20:21]\n"
    "v_pk_add_f32 v[18:19], v[18:19], v[22:23]\n"
    "v_pk_add_f32 v[16:17], v[16:17], v[18:19]\n"
    "v_cvt_pk_bf16_f32 v16, 0, v16\n"
    "v_cvt_pk_bf16_f32 v17, 0, v17\n"
    "v_add_f32 v16, s43, v16\n"
    "v_add_f32 v17, s43, v17\n"
    "v_cvt_pk_bf16_f32 v16, v16, v17\n"
    "v_min_u32 v16, -2, v16\n"
    "buffer_store_dword v16, v9, s[16:19], s64 offen sc1\n"
    "s_mov_b64 exec, -1\n"
    ".Ltab_x:\n"
    "s_swappc_b64 s[36:37], s[34:35]\n"
    "s_swappc_b64 s[36:37], s[34:35]\n"
    "s_swappc_b64 s[36:37], s[34:35]\n"
    "s_swappc_b64 s[36:37], s[34:35]\n"
    "s_swappc_b64 s[36:37], s[34:35]\n"
    "s_cmp_gt_u32 s28, 1\n"
    "s_cbranch_scc1 .Lq0_x\n"
    "s_waitcnt vmcnt(40)\n"
    ".Lq0_chk:\n"
    "v_accvgpr_read_b32 v44, a240\n"
    "v_accvgpr_read_b32 v45, a241\n"
    "v_accvgpr_read_b32 v46, a242\n"
    "v_accvgpr_read_b32 v47, a243\n"
    "v_accvgpr_read_b32 v48, a244\n"
    "v_accvgpr_read_b32 v49, a245\n"
    "v_accvgpr_read_b32 v50, a246\n"
    "v_accvgpr_read_b32 v51, a247\n"
    "v_accvgpr_read_b32 v52, a248\n"
    "v_accvgpr_read_b32 v53, a249\n"
    "v_accvgpr_read_b32 v54, a250\n"
    "v_accvgpr_read_b32 v55, a251\n"
    "v_accvgpr_read_b32 v56, a252\n"
    "v_accvgpr_read_b32 v57, a253\n"
    "v_accvgpr_read_b32 v58, a254\n"
    "v_accvgpr_read_b32 v59, a255\n"
    "v_max3_u32 v9, v44, v45, v46\n"
    "v_max_u32 v9, v9, v47\n"
    "v_max3_u32 v9, v9, v48, v49\n"
    "v_max3_u32 v9, v9, v50, v51\n"
    "v_max3_u32 v9, v9, v52, v53\n"
    "v_max3_u32 v9, v9, v54, v55\n"
    "v_max3_u32 v9, v9, v56, v57\n"
    "v_max3_u32 v9, v9, v58, v59\n"
    "v_cmp_eq_u32 vcc, -1, v9\n"
    "s_cbranch_vccnz .Lq0_re\n"
    ".Lq0_ok:\n"
    "v_pk_add_f32 v[44:45], v[44:45], v[48:49]\n"
    "v_pk_add_f32 v[46:47], v[46:47], v[50:51]\n"
    "v_pk_add_f32 v[52:53], v[52:53], v[56:57]\n"
    "v_pk_add_f32 v[54:55], v[54:55], v[58:59]\n"
    "v_pk_add_f32 v[44:45], v[44:45], v[52:53]\n"
    "v_pk_add_f32 v[46:47], v[46:47], v[54:55]\n"
    "v_lshrrev_b32 v9, 1, v4\n"
    "v_and_b32 v9, 0x160, v9\n"
    "v_bfe_u32 v10, v4, 8, 1\n"
    "v_lshl_or_b32 v9, v10, 4, v9\n"
    "v_bfe_u32 v10, v4, 4, 2\n"
    "v_lshl_or_b32 v9, v10, 2, v9\n"
    "s_cmp_eq_u32 s28, 0\n"
    "s_mov_b32 s64, 97536\n"
    "s_cselect_b32 s64, 158720, s64\n"
    "v_add_u32 v9, s64, v9\n"
    "v_cvt_pk_bf16_f32 v44, v44, v45\n"
    "v_cvt_pk_bf16_f32 v45, v46, v47\n"
    "ds_write2_b32 v9, v44, v45 offset1:32\n"
    ".Lq0_x:\n"
    "s_waitcnt lgkmcnt(0)\n"
    "s_barrier\n"
    "s_mov_b32 s45, 0\n"
    "s_swappc_b64 s[36:37], s[34:35]\n"
    "s_getpc_b64 s[96:97]\n"
    ".Lpc538_.Lwof:\n"
    "s_add_u32 s96, s96, .Lwof-.Lpc538_.Lwof\n"
    "s_addc_u32 s97, s97, 0\n"
    ".Lloop:\n"
    "s_bitcmp0_b32 s41, 0\n"
    "v_lshrrev_b32 v25, 8, v4\n"
    "v_and_b32 v26, 0xf0, v4\n"
    "s_cbranch_scc1 .Lsp0\n"
    "s_lshl_b32 s70, s28, 8\n"
    "v_lshl_or_b32 v24, v25, 10, v26\n"
    "s_add_u32 s70, s70, s50\n"
    "v_mov_b64 v[10:11], -1\n"
    "v_mov_b64 v[12:13], -1\n"
    "buffer_load_dwordx4 v[10:13], v24, s[16:19], s70 offen sc1\n"
    ".Lsp0:\n"
    "s_add_u32 s64, s42, 11264\n"
    "s_cmp_eq_u32 s41, 0\n"
    "s_cselect_b32 s42, s42, 49152\n"
    "s_cmp_eq_u32 s41, 2\n"
    "s_cselect_b32 s64, 97536, s64\n"
    "s_cmp_gt_u32 s41, 1\n"
    "s_cselect_b32 s65, 98304, 0\n"
    "s_add_u32 s65, s65, s73\n"
    "v_bfe_u32 v6, v4, 4, 3\n"
    "v_xor_b32 v9, v6, v25\n"
    "v_lshl_or_b32 v9, v6, 3, v9\n"
    "v_and_b32 v6, 0x80, v4\n"
    "v_or_b32 v9, v9, v6\n"
    "v_lshl_add_u32 v9, v9, 4, s65\n"
    "v_xor_b32 v8, 64, v9\n"
    "ds_read_b128 v[48:51], v9 offset:0\n"
    "ds_read_b128 v[52:55], v8 offset:0\n"
    "ds_read_b128 v[56:59], v9 offset:1024\n"
    "ds_read_b128 v[60:63], v8 offset:1024\n"
    "v_bfe_i32 v6, v4, 7, 1\n"
    "v_xor_b32 v6, 0xffff, v6\n"
    "v_and_b32 v7, 16, v4\n"
    "v_mul_u32_u24 v7, 0xaaa, v7\n"
    "v_lshrrev_b32 v27, 8, v7\n"
    "v_or_b32 v7, v7, v27\n"
    "v_or_b32 v7, 0x4444, v7\n"
    "v_bfe_u32 v28, v4, 4, 2\n"
    "v_lshlrev_b32 v8, 7, v28\n"
    "v_lshl_or_b32 v8, v25, 5, v8\n"
    "v_add_u32 v8, s64, v8\n"
    "v_and_b32 v27, 0xc0, v4\n"
    "v_cmp_eq_u32 vcc, 0, v27\n"
    "v_mov_b32 v29, 98048\n"
    "v_mov_b32 v15, 0x3e0293ee\n"
    "v_cndmask_b32 v8, v29, v8, vcc\n"
    "v_mul_u32_u24 v25, 0x42, v25\n"
    "v_and_b32 v25, 0x82, v25\n"
    "v_lshrrev_b32 v27, 1, v27\n"
    "v_lshl_or_b32 v27, v28, 2, v27\n"
    "v_or_b32 v25, v25, v27\n"
    "s_mul_i32 s65, s28, 0x90\n"
    "s_and_b32 s65, s65, 0x110\n"
    "s_add_u32 s64, s64, s65\n"
    "v_add_u32 v25, s64, v25\n"
    "v_lshrrev_b32 v14, 2, v4\n"
    "s_add_u32 s74, s42, s73\n"
    "s_and_b32 s65, s28, 1\n"
    "s_lshl_b32 s65, s65, 11\n"
    "s_lshr_b32 s66, s28, 1\n"
    "s_lshl_b32 s67, s66, 9\n"
    "s_add_u32 s67, s67, s42\n"
    "s_add_u32 s75, s65, s67\n"
    "s_add_u32 s76, s67, 1520\n"
    "s_lshl_b32 s64, s41, 4\n"
    "s_add_u32 s64, s64, s30\n"
    "s_lshl_b32 s64, s64, 4\n"
    "s_add_u32 s64, s64, s31\n"
    "s_mul_i32 s64, s64, 1152\n"
    "s_add_u32 s64, s64, 4096\n"
    "s_lshl_b32 s65, s28, 8\n"
    "s_add_u32 s49, s64, s65\n"
    "s_lshl_b32 s66, s66, 3\n"
    "s_add_u32 s71, s64, s66\n"
    "v_mov_b64 v[32:33], 0\n"
    "v_mov_b64 v[34:35], 0\n"
    "v_mov_b64 v[36:37], 0\n"
    "v_mov_b64 v[38:39], 0\n"
    "v_mov_b64 v[40:41], 0\n"
    "v_mov_b64 v[42:43], 0\n"
    "v_mov_b64 v[44:45], 0\n"
    "v_mov_b64 v[46:47], 0\n"
    "s_bitcmp0_b32 s41, 0\n"
    "s_cbranch_scc1 .Lscore\n"
    ".Lqp:\n"
    "v_max3_u32 v26, v10, v11, v12\n"
    "v_max_u32 v26, v26, v13\n"
    "v_mov_b64 v[16:17], v[10:11]\n"
    "v_mov_b64 v[18:19], v[12:13]\n"
    "v_cmp_eq_u32 vcc, -1, v26\n"
    "s_nop 0\n"
    "v_permlane32_swap_b32 v16, v18\n"
    "v_permlane32_swap_b32 v17, v19\n"
    "v_pk_add_f32 v[16:17], v[16:17], v[18:19]\n"
    "s_cbranch_vccnz .Lqp_re\n"
    ".Lqp_ok:\n"
    "s_nop 0\n"
    "v_permlane16_swap_b32 v16, v17\n"
    "v_add_f32 v16, v16, v17\n"
    "v_cvt_pk_bf16_f32 v16, v16, v16\n"
    "ds_write_b16 v25, v16\n"
    "s_waitcnt lgkmcnt(0)\n"
    "s_barrier\n"
    ".Lscore:\n"
    "ds_read_b128 v[16:19], v8\n"
    "ds_read_b128 v[20:23], v8 offset:16\n"
    "v_xor_b32 v8, 64, v9\n"
    "s_cmp_eq_u32 s41, 1\n"
    "s_waitcnt lgkmcnt(0)\n"
    "v_smfmac_f32_16x16x64_bf16 v[32:35], v[16:19], v[64:71], v7\n"
    "v_mov_b32_dpp v24, v16 row_ror:4 row_mask:0xf bank_mask:0xf\n"
    "v_smfmac_f32_16x16x64_bf16 v[32:35], v[20:23], v[72:79], v7\n"
    "v_mov_b32_dpp v25, v17 row_ror:4 row_mask:0xf bank_mask:0xf\n"
    "v_smfmac_f32_16x16x64_bf16 v[36:39], v[16:19], v[80:87], v7\n"
    "v_mov_b32_dpp v26, v18 row_ror:4 row_mask:0xf bank_mask:0xf\n"
    "v_smfmac_f32_16x16x64_bf16 v[36:39], v[20:23], v[88:95], v7\n"
    "v_mov_b32_dpp v27, v19 row_ror:4 row_mask:0xf bank_mask:0xf\n"
    "v_smfmac_f32_16x16x64_bf16 v[40:43], v[16:19], v[96:103], v7\n"
    "v_mov_b32_dpp v28, v20 row_ror:4 row_mask:0xf bank_mask:0xf\n"
    "v_smfmac_f32_16x16x64_bf16 v[40:43], v[20:23], v[104:111], v7\n"
    "v_mov_b32_dpp v29, v21 row_ror:4 row_mask:0xf bank_mask:0xf\n"
    "s_cbranch_scc1 .Lhook\n"
    ".Lhook_r:\n"
    "v_smfmac_f32_16x16x64_bf16 v[44:47], v[16:19], v[48:55], v7\n"
    "v_mov_b32_dpp v30, v22 row_ror:4 row_mask:0xf bank_mask:0xf\n"
    "ds_read_b128 v[48:51], v9 offset:16384\n"
    "ds_read_b128 v[52:55], v8 offset:16384\n"
    "v_smfmac_f32_16x16x64_bf16 v[44:47], v[20:23], v[56:63], v7\n"
    "v_mov_b32_dpp v31, v23 row_ror:4 row_mask:0xf bank_mask:0xf\n"
    "ds_read_b128 v[56:59], v9 offset:17408\n"
    "ds_read_b128 v[60:63], v8 offset:17408\n"
    "v_smfmac_f32_16x16x64_bf16 v[32:35], v[24:27], v[112:119], v7\n"
    "v_mov_b32_dpp v16, v24 row_ror:4 row_mask:0xf bank_mask:0xf\n"
    "v_smfmac_f32_16x16x64_bf16 v[32:35], v[28:31], v[120:127], v7\n"
    "v_mov_b32_dpp v17, v25 row_ror:4 row_mask:0xf bank_mask:0xf\n"
    "v_smfmac_f32_16x16x64_bf16 v[36:39], v[24:27], v[128:135], v7\n"
    "v_mov_b32_dpp v18, v26 row_ror:4 row_mask:0xf bank_mask:0xf\n"
    "v_smfmac_f32_16x16x64_bf16 v[36:39], v[28:31], v[136:143], v7\n"
    "v_mov_b32_dpp v19, v27 row_ror:4 row_mask:0xf bank_mask:0xf\n"
    "v_smfmac_f32_16x16x64_bf16 v[40:43], v[24:27], v[144:151], v7\n"
    "v_mov_b32_dpp v20, v28 row_ror:4 row_mask:0xf bank_mask:0xf\n"
    "v_smfmac_f32_16x16x64_bf16 v[40:43], v[28:31], v[152:159], v7\n"
    "v_mov_b32_dpp v21, v29 row_ror:4 row_mask:0xf bank_mask:0xf\n"
    "s_waitcnt lgkmcnt(0)\n"
    "v_smfmac_f32_16x16x64_bf16 v[44:47], v[24:27], v[48:55], v7\n"
    "v_mov_b32_dpp v22, v30 row_ror:4 row_mask:0xf bank_mask:0xf\n"
    "ds_read_b128 v[48:51], v9 offset:32768\n"
    "ds_read_b128 v[52:55], v8 offset:32768\n"
    "v_smfmac_f32_16x16x64_bf16 v[44:47], v[28:31], v[56:63], v7\n"
    "v_mov_b32_dpp v23, v31 row_ror:4 row_mask:0xf bank_mask:0xf\n"
    "ds_read_b128 v[56:59], v9 offset:33792\n"
    "ds_read_b128 v[60:63], v8 offset:33792\n"
    "v_smfmac_f32_16x16x64_bf16 v[32:35], v[16:19], v[160:167], v7\n"
    "v_mov_b32_dpp v24, v16 row_ror:4 row_mask:0xf bank_mask:0xf\n"
    "v_smfmac_f32_16x16x64_bf16 v[32:35], v[20:23], v[168:175], v7\n"
    "v_mov_b32_dpp v25, v17 row_ror:4 row_mask:0xf bank_mask:0xf\n"
    "v_smfmac_f32_16x16x64_bf16 v[36:39], v[16:19], v[176:183], v7\n"
    "v_mov_b32_dpp v26, v18 row_ror:4 row_mask:0xf bank_mask:0xf\n"
    "v_smfmac_f32_16x16x64_bf16 v[36:39], v[20:23], v[184:191], v7\n"
    "v_mov_b32_dpp v27, v19 row_ror:4 row_mask:0xf bank_mask:0xf\n"
    "v_smfmac_f32_16x16x64_bf16 v[40:43], v[16:19], v[192:199], v7\n"
    "v_mov_b32_dpp v28, v20 row_ror:4 row_mask:0xf bank_mask:0xf\n"
    "v_smfmac_f32_16x16x64_bf16 v[40:43], v[20:23], v[200:207], v7\n"
    "v_mov_b32_dpp v29, v21 row_ror:4 row_mask:0xf bank_mask:0xf\n"
    "s_waitcnt lgkmcnt(0)\n"
    "v_smfmac_f32_16x16x64_bf16 v[44:47], v[16:19], v[48:55], v7\n"
    "v_mov_b32_dpp v30, v22 row_ror:4 row_mask:0xf bank_mask:0xf\n"
    "ds_read_b128 v[48:51], v9 offset:49152\n"
    "ds_read_b128 v[52:55], v8 offset:49152\n"
    "v_smfmac_f32_16x16x64_bf16 v[44:47], v[20:23], v[56:63], v7\n"
    "v_mov_b32_dpp v31, v23 row_ror:4 row_mask:0xf bank_mask:0xf\n"
    "ds_read_b128 v[56:59], v9 offset:50176\n"
    "ds_read_b128 v[60:63], v8 offset:50176\n"
    "v_smfmac_f32_16x16x64_bf16 v[32:35], v[24:27], v[208:215], v7\n"
    "v_smfmac_f32_16x16x64_bf16 v[32:35], v[28:31], v[216:223], v7\n"
    "v_smfmac_f32_16x16x64_bf16 v[36:39], v[24:27], v[224:231], v7\n"
    "v_smfmac_f32_16x16x64_bf16 v[36:39], v[28:31], v[232:239], v7\n"
    "v_smfmac_f32_16x16x64_bf16 v[40:43], v[24:27], v[240:247], v7\n"
    "v_smfmac_f32_16x16x64_bf16 v[40:43], v[28:31], v[248:255], v7\n"
    "s_waitcnt lgkmcnt(0)\n"
    "v_smfmac_f32_16x16x64_bf16 v[44:47], v[24:27], v[48:55], v7\n"
    "v_smfmac_f32_16x16x64_bf16 v[44:47], v[28:31], v[56:63], v7\n"
    "s_nop 0\n"
    "v_add_f32 v16, v32, v33\n"
    "v_add_f32 v20, v34, v35\n"
    "v_add_f32 v17, v36, v37\n"
    "v_add_f32 v21, v38, v39\n"
    "v_add_f32 v18, v40, v41\n"
    "v_add_f32 v22, v42, v43\n"
    "v_max3_f32 v29, v16, v17, v18\n"
    "v_max3_f32 v30, v20, v21, v22\n"
    "v_add_f32 v19, v44, v45\n"
    "v_add_f32 v23, v46, v47\n"
    "v_max_f32 v29, v29, v19\n"
    "v_max_f32 v30, v30, v23\n"
    "v_mov_b32 v32, 0\n"
    "v_mov_b32 v33, 0\n"
    "v_permlane32_swap_b32 v29, v30\n"
    "v_max_f32 v29, v29, v30\n"
    "v_mov_b32 v34, 0\n"
    "v_mov_b32 v35, 0\n"
    "v_max_f32_dpp v29, v29, v29 quad_perm:[1,0,3,2] row_mask:0xf bank_mask:0xf\n"
    "v_mov_b32 v36, 0\n"
    "v_mov_b32 v37, 0\n"
    "v_max_f32_dpp v29, v29, v29 quad_perm:[2,3,0,1] row_mask:0xf bank_mask:0xf\n"
    "v_mov_b32 v38, 0\n"
    "v_mov_b32 v39, 0\n"
    "v_max_f32_dpp v29, v29, v29 row_half_mirror row_mask:0xf bank_mask:0xf\n"
    "s_nop 1\n"
    "v_max_f32_dpp v29, v29, v29 row_mirror row_mask:0xf bank_mask:0xf\n"
    "s_nop 1\n"
    "v_max_f32_dpp v29, v29, v29 row_bcast:15 row_mask:0xa bank_mask:0xf\n"
    "v_mul_f32 v29, v29, v15\n"
    "s_nop 0\n"
    "v_readlane_b32 s64, v29, 31\n"
    "v_readlane_b32 s65, v29, 63\n"
    "s_nop 1\n"
    "v_pk_fma_f32 v[16:17], v[16:17], v[14:15], s[64:65] op_sel:[0,1,0] op_sel_hi:[1,1,0] neg_lo:[0,0,1] neg_hi:[0,0,1]\n"
    "v_pk_fma_f32 v[18:19], v[18:19], v[14:15], s[64:65] op_sel:[0,1,0] op_sel_hi:[1,1,0] neg_lo:[0,0,1] neg_hi:[0,0,1]\n"
    "v_pk_fma_f32 v[20:21], v[20:21], v[14:15], s[64:65] op_sel:[0,1,1] op_sel_hi:[1,1,1] neg_lo:[0,0,1] neg_hi:[0,0,1]\n"
    "v_pk_fma_f32 v[22:23], v[22:23], v[14:15], s[64:65] op_sel:[0,1,1] op_sel_hi:[1,1,1] neg_lo:[0,0,1] neg_hi:[0,0,1]\n"
    "v_exp_f32 v16, v16\n"
    "v_exp_f32 v20, v20\n"
    "v_exp_f32 v17, v17\n"
    "v_exp_f32 v21, v21\n"
    "v_exp_f32 v18, v18\n"
    "v_exp_f32 v22, v22\n"
    "v_exp_f32 v19, v19\n"
    "v_exp_f32 v23, v23\n"
    "v_cvt_pk_bf16_f32 v24, v16, v20\n"
    "v_cvt_pk_bf16_f32 v25, v17, v21\n"
    "v_cvt_pk_bf16_f32 v26, v18, v22\n"
    "v_cvt_pk_bf16_f32 v27, v19, v23\n"
    "s_bitcmp0_b32 s41, 0\n"
    "v_pk_add_f32 v[16:17], v[16:17], v[18:19]\n"
    "v_pk_add_f32 v[20:21], v[20:21], v[22:23]\n"
    "v_add_f32 v28, v16, v17\n"
    "v_add_f32 v30, v20, v21\n"
    "s_cbranch_scc1 .Lvrest\n"
    ".Lpv:\n"
    "v_and_b32_dpp v16, v24, v6 row_newbcast:0 row_mask:0xf bank_mask:0xf\n"
    "v_and_b32_dpp v17, v24, v6 row_newbcast:1 row_mask:0xf bank_mask:0xf\n"
    "v_and_b32_dpp v18, v24, v6 row_newbcast:8 row_mask:0xf bank_mask:0xf\n"
    "v_and_b32_dpp v19, v24, v6 row_newbcast:9 row_mask:0xf bank_mask:0xf\n"
    "v_and_b32_dpp v20, v24, v6 row_newbcast:2 row_mask:0xf bank_mask:0xf\n"
    "v_and_b32_dpp v21, v24, v6 row_newbcast:3 row_mask:0xf bank_mask:0xf\n"
    "v_smfmac_f32_16x16x64_bf16 v[32:35], v[16:19], a[0:7], v5\n"
    "v_and_b32_dpp v22, v24, v6 row_newbcast:10 row_mask:0xf bank_mask:0xf\n"
    "v_and_b32_dpp v23, v24, v6 row_newbcast:11 row_mask:0xf bank_mask:0xf\n"
    "v_add_u32 v40, s74, v4\n"
    "v_smfmac_f32_16x16x64_bf16 v[36:39], v[16:19], a[8:15], v5\n"
    "v_and_b32_dpp v16, v24, v6 row_newbcast:4 row_mask:0xf bank_mask:0xf\n"
    "v_and_b32_dpp v17, v24, v6 row_newbcast:5 row_mask:0xf bank_mask:0xf\n"
    "v_smfmac_f32_16x16x64_bf16 v[32:35], v[20:23], a[16:23], v5\n"
    "v_and_b32_dpp v18, v24, v6 row_newbcast:12 row_mask:0xf bank_mask:0xf\n"
    "v_and_b32_dpp v19, v24, v6 row_newbcast:13 row_mask:0xf bank_mask:0xf\n"
    "v_add_u32 v41, s75, v14\n"
    "v_smfmac_f32_16x16x64_bf16 v[36:39], v[20:23], a[24:31], v5\n"
    "v_and_b32_dpp v20, v24, v6 row_newbcast:6 row_mask:0xf bank_mask:0xf\n"
    "v_and_b32_dpp v21, v24, v6 row_newbcast:7 row_mask:0xf bank_mask:0xf\n"
    "v_permlane32_swap_b32 v28, v30\n"
    "v_smfmac_f32_16x16x64_bf16 v[32:35], v[16:19], a[32:39], v5\n"
    "v_and_b32_dpp v22, v24, v6 row_newbcast:14 row_mask:0xf bank_mask:0xf\n"
    "v_and_b32_dpp v23, v24, v6 row_newbcast:15 row_mask:0xf bank_mask:0xf\n"
    "v_bfe_u32 v42, v4, 4, 2\n"
    "v_smfmac_f32_16x16x64_bf16 v[36:39], v[16:19], a[40:47], v5\n"
    "v_and_b32_dpp v16, v25, v6 row_newbcast:0 row_mask:0xf bank_mask:0xf\n"
    "v_and_b32_dpp v17, v25, v6 row_newbcast:1 row_mask:0xf bank_mask:0xf\n"
    "v_add_f32 v28, v28, v30\n"
    "v_smfmac_f32_16x16x64_bf16 v[32:35], v[20:23], a[48:55], v5\n"
    "v_and_b32_dpp v18, v25, v6 row_newbcast:8 row_mask:0xf bank_mask:0xf\n"
    "v_and_b32_dpp v19, v25, v6 row_newbcast:9 row_mask:0xf bank_mask:0xf\n"
    "v_lshl_add_u32 v42, v42, 12, s76\n"
    "v_smfmac_f32_16x16x64_bf16 v[36:39], v[20:23], a[56:63], v5\n"
    "v_and_b32_dpp v20, v25, v6 row_newbcast:2 row_mask:0xf bank_mask:0xf\n"
    "v_and_b32_dpp v21, v25, v6 row_newbcast:3 row_mask:0xf bank_mask:0xf\n"
    "v_add_f32_dpp v28, v28, v28 quad_perm:[1,0,3,2] row_mask:0xf bank_mask:0xf\n"
    "v_smfmac_f32_16x16x64_bf16 v[32:35], v[16:19], a[64:71], v5\n"
    "v_and_b32_dpp v22, v25, v6 row_newbcast:10 row_mask:0xf bank_mask:0xf\n"
    "v_and_b32_dpp v23, v25, v6 row_newbcast:11 row_mask:0xf bank_mask:0xf\n"
    "v_smfmac_f32_16x16x64_bf16 v[36:39], v[16:19], a[72:79], v5\n"
    "v_and_b32_dpp v16, v25, v6 row_newbcast:4 row_mask:0xf bank_mask:0xf\n"
    "v_and_b32_dpp v17, v25, v6 row_newbcast:5 row_mask:0xf bank_mask:0xf\n"
    "v_add_f32_dpp v28, v28, v28 quad_perm:[2,3,0,1] row_mask:0xf bank_mask:0xf\n"
    "v_smfmac_f32_16x16x64_bf16 v[32:35], v[20:23], a[80:87], v5\n"
    "v_and_b32_dpp v18, v25, v6 row_newbcast:12 row_mask:0xf bank_mask:0xf\n"
    "v_and_b32_dpp v19, v25, v6 row_newbcast:13 row_mask:0xf bank_mask:0xf\n"
    "v_smfmac_f32_16x16x64_bf16 v[36:39], v[20:23], a[88:95], v5\n"
    "v_and_b32_dpp v20, v25, v6 row_newbcast:6 row_mask:0xf bank_mask:0xf\n"
    "v_and_b32_dpp v21, v25, v6 row_newbcast:7 row_mask:0xf bank_mask:0xf\n"
    "v_add_f32_dpp v28, v28, v28 row_half_mirror row_mask:0xf bank_mask:0xf\n"
    "v_smfmac_f32_16x16x64_bf16 v[32:35], v[16:19], a[96:103], v5\n"
    "v_and_b32_dpp v22, v25, v6 row_newbcast:14 row_mask:0xf bank_mask:0xf\n"
    "v_and_b32_dpp v23, v25, v6 row_newbcast:15 row_mask:0xf bank_mask:0xf\n"
    "v_smfmac_f32_16x16x64_bf16 v[36:39], v[16:19], a[104:111], v5\n"
    "v_and_b32_dpp v16, v26, v6 row_newbcast:0 row_mask:0xf bank_mask:0xf\n"
    "v_and_b32_dpp v17, v26, v6 row_newbcast:1 row_mask:0xf bank_mask:0xf\n"
    "v_add_f32_dpp v28, v28, v28 row_mirror row_mask:0xf bank_mask:0xf\n"
    "v_smfmac_f32_16x16x64_bf16 v[32:35], v[20:23], a[112:119], v5\n"
    "v_and_b32_dpp v18, v26, v6 row_newbcast:8 row_mask:0xf bank_mask:0xf\n"
    "v_and_b32_dpp v19, v26, v6 row_newbcast:9 row_mask:0xf bank_mask:0xf\n"
    "v_smfmac_f32_16x16x64_bf16 v[36:39], v[20:23], a[120:127], v5\n"
    "v_and_b32_dpp v20, v26, v6 row_newbcast:2 row_mask:0xf bank_mask:0xf\n"
    "v_and_b32_dpp v21, v26, v6 row_newbcast:3 row_mask:0xf bank_mask:0xf\n"
    "v_add_f32_dpp v28, v28, v28 row_bcast:15 row_mask:0xa bank_mask:0xf\n"
    "v_smfmac_f32_16x16x64_bf16 v[32:35], v[16:19], a[128:135], v5\n"
    "v_and_b32_dpp v22, v26, v6 row_newbcast:10 row_mask:0xf bank_mask:0xf\n"
    "v_and_b32_dpp v23, v26, v6 row_newbcast:11 row_mask:0xf bank_mask:0xf\n"
    "v_smfmac_f32_16x16x64_bf16 v[36:39], v[16:19], a[136:143], v5\n"
    "v_and_b32_dpp v16, v26, v6 row_newbcast:4 row_mask:0xf bank_mask:0xf\n"
    "v_and_b32_dpp v17, v26, v6 row_newbcast:5 row_mask:0xf bank_mask:0xf\n"
    "ds_write_b64 v40, v[28:29] offset:1024\n"
    "v_smfmac_f32_16x16x64_bf16 v[32:35], v[20:23], a[144:151], v5\n"
    "v_and_b32_dpp v18, v26, v6 row_newbcast:12 row_mask:0xf bank_mask:0xf\n"
    "v_and_b32_dpp v19, v26, v6 row_newbcast:13 row_mask:0xf bank_mask:0xf\n"
    "v_smfmac_f32_16x16x64_bf16 v[36:39], v[20:23], a[152:159], v5\n"
    "v_and_b32_dpp v20, v26, v6 row_newbcast:6 row_mask:0xf bank_mask:0xf\n"
    "v_and_b32_dpp v21, v26, v6 row_newbcast:7 row_mask:0xf bank_mask:0xf\n"
    "v_smfmac_f32_16x16x64_bf16 v[32:35], v[16:19], a[160:167], v5\n"
    "v_and_b32_dpp v22, v26, v6 row_newbcast:14 row_mask:0xf bank_mask:0xf\n"
    "v_and_b32_dpp v23, v26, v6 row_newbcast:15 row_mask:0xf bank_mask:0xf\n"
    "v_smfmac_f32_16x16x64_bf16 v[36:39], v[16:19], a[168:175], v5\n"
    "v_and_b32_dpp v16, v27, v6 row_newbcast:0 row_mask:0xf bank_mask:0xf\n"
    "v_and_b32_dpp v17, v27, v6 row_newbcast:1 row_mask:0xf bank_mask:0xf\n"
    "s_cbranch_scc1 .Lpvw11\n"
    ".Lpvr11:\n"
    "v_smfmac_f32_16x16x64_bf16 v[32:35], v[20:23], a[176:183], v5\n"
    "v_and_b32_dpp v18, v27, v6 row_newbcast:8 row_mask:0xf bank_mask:0xf\n"
    "v_and_b32_dpp v19, v27, v6 row_newbcast:9 row_mask:0xf bank_mask:0xf\n"
    "v_smfmac_f32_16x16x64_bf16 v[36:39], v[20:23], a[184:191], v5\n"
    "v_and_b32_dpp v20, v27, v6 row_newbcast:2 row_mask:0xf bank_mask:0xf\n"
    "v_and_b32_dpp v21, v27, v6 row_newbcast:3 row_mask:0xf bank_mask:0xf\n"
    "s_cbranch_scc1 .Lpvw12\n"
    ".Lpvr12:\n"
    "v_smfmac_f32_16x16x64_bf16 v[32:35], v[16:19], a[192:199], v5\n"
    "v_and_b32_dpp v22, v27, v6 row_newbcast:10 row_mask:0xf bank_mask:0xf\n"
    "v_and_b32_dpp v23, v27, v6 row_newbcast:11 row_mask:0xf bank_mask:0xf\n"
    "v_smfmac_f32_16x16x64_bf16 v[36:39], v[16:19], a[200:207], v5\n"
    "v_and_b32_dpp v16, v27, v6 row_newbcast:4 row_mask:0xf bank_mask:0xf\n"
    "v_and_b32_dpp v17, v27, v6 row_newbcast:5 row_mask:0xf bank_mask:0xf\n"
    "s_cbranch_scc1 .Lpvw13\n"
    ".Lpvr13:\n"
    "v_smfmac_f32_16x16x64_bf16 v[32:35], v[20:23], a[208:215], v5\n"
    "v_and_b32_dpp v18, v27, v6 row_newbcast:12 row_mask:0xf bank_mask:0xf\n"
    "v_and_b32_dpp v19, v27, v6 row_newbcast:13 row_mask:0xf bank_mask:0xf\n"
    "v_smfmac_f32_16x16x64_bf16 v[36:39], v[20:23], a[216:223], v5\n"
    "v_and_b32_dpp v20, v27, v6 row_newbcast:6 row_mask:0xf bank_mask:0xf\n"
    "v_and_b32_dpp v21, v27, v6 row_newbcast:7 row_mask:0xf bank_mask:0xf\n"
    "s_cbranch_scc1 .Lpvw14\n"
    ".Lpvr14:\n"
    "v_smfmac_f32_16x16x64_bf16 v[32:35], v[16:19], a[224:231], v5\n"
    "v_and_b32_dpp v22, v27, v6 row_newbcast:14 row_mask:0xf bank_mask:0xf\n"
    "v_and_b32_dpp v23, v27, v6 row_newbcast:15 row_mask:0xf bank_mask:0xf\n"
    "v_smfmac_f32_16x16x64_bf16 v[36:39], v[16:19], a[232:239], v5\n"
    "s_cbranch_scc1 .Lpvw15\n"
    ".Lpvr15:\n"
    "v_smfmac_f32_16x16x64_bf16 v[32:35], v[20:23], a[240:247], v5\n"
    "v_smfmac_f32_16x16x64_bf16 v[36:39], v[20:23], a[248:255], v5\n"
    "s_nop 6\n"
    "ds_write_b128 v40, v[32:35]\n"
    "ds_write_b128 v40, v[36:39] offset:2048\n"
    "s_waitcnt lgkmcnt(0)\n"
    "s_barrier\n"
    "ds_read_b64 v[16:17], v42\n"
    "ds_read2st64_b32 v[20:21], v41 offset1:16\n"
    "ds_read2st64_b32 v[22:23], v41 offset0:32 offset1:48\n"
    "s_cmp_eq_u32 s41, 1\n"
    "s_waitcnt lgkmcnt(2)\n"
    "v_max_f32_dpp v18, v17, v17 quad_perm:[1,0,3,2] row_mask:0xf bank_mask:0xf\n"
    "s_nop 1\n"
    "v_max_f32_dpp v18, v18, v18 quad_perm:[2,3,0,1] row_mask:0xf bank_mask:0xf\n"
    "v_sub_f32 v17, v17, v18\n"
    "v_exp_f32 v17, v17\n"
    "v_min_u32 v18, -2, v18\n"
    "v_mul_f32 v19, v16, v17\n"
    "s_waitcnt lgkmcnt(0)\n"
    "v_mul_f32_dpp v20, v17, v20 quad_perm:[0,0,0,0] row_mask:0xf bank_mask:0xf\n"
    "v_fmac_f32_dpp v20, v17, v21 quad_perm:[1,1,1,1] row_mask:0xf bank_mask:0xf\n"
    "v_fmac_f32_dpp v20, v17, v22 quad_perm:[2,2,2,2] row_mask:0xf bank_mask:0xf\n"
    "v_fmac_f32_dpp v20, v17, v23 quad_perm:[3,3,3,3] row_mask:0xf bank_mask:0xf\n"
    "v_add_f32_dpp v19, v19, v19 quad_perm:[1,0,3,2] row_mask:0xf bank_mask:0xf\n"
    "v_min_u32 v20, -2, v20\n"
    "buffer_store_dword v20, v14, s[16:19], s49 offen sc1\n"
    "v_add_f32_dpp v19, v19, v19 quad_perm:[2,3,0,1] row_mask:0xf bank_mask:0xf\n"
    "v_min_u32 v19, -2, v19\n"
    "buffer_store_dwordx2 v[18:19], off, s[16:19], s71 offset:1024 sc1\n"
    "s_cbranch_scc1 .Ldrv2\n"
    "s_waitcnt vmcnt(2)\n"
    "s_mov_b32 s66, s41\n"
    "s_mov_b32 s67, s31\n"
    "s_cmp_gt_u32 s31, 8\n"
    "s_cbranch_scc1 .Ltask2\n"
    ".Ltask:\n"
    "s_lshl_b32 s64, s66, 4\n"
    "s_add_u32 s64, s64, s30\n"
    "s_mul_i32 s65, s64, 18432\n"
    "s_add_u32 s65, s65, 4096\n"
    "s_lshr_b32 s68, s28, 1\n"
    "s_lshl_b32 s68, s68, 3\n"
    "s_add_u32 s79, s65, s68\n"
    "s_lshl_b32 s68, s28, 8\n"
    "s_add_u32 s75, s65, s68\n"
    "v_lshrrev_b32 v12, 8, v4\n"
    "v_lshrrev_b32 v10, 4, v4\n"
    "v_add_u32 v10, v10, v12\n"
    "v_and_b32 v10, 15, v10\n"
    "v_mul_u32_u24 v10, 1152, v10\n"
    "v_mul_u32_u24 v11, 1152, v12\n"
    "v_and_b32 v12, 0xf0, v4\n"
    "v_add_u32 v11, v11, v12\n"
    "s_add_u32 s76, s75, 4608\n"
    "s_add_u32 s77, s75, 9216\n"
    "s_add_u32 s78, s75, 13824\n"
    "v_mov_b64 v[16:17], -1\n"
    "v_mov_b64 v[18:19], -1\n"
    "v_mov_b64 v[20:21], -1\n"
    "v_mov_b64 v[22:23], -1\n"
    "v_mov_b64 v[24:25], -1\n"
    "v_mov_b64 v[26:27], -1\n"
    "v_mov_b64 v[28:29], -1\n"
    "v_mov_b64 v[30:31], -1\n"
    "v_mov_b64 v[32:33], -1\n"
    "buffer_load_dwordx4 v[16:19], v11, s[16:19], s75 offen sc1\n"
    "buffer_load_dwordx4 v[20:23], v11, s[16:19], s76 offen sc1\n"
    "buffer_load_dwordx4 v[24:27], v11, s[16:19], s77 offen sc1\n"
    "buffer_load_dwordx4 v[28:31], v11, s[16:19], s78 offen sc1\n"
    "buffer_load_dwordx2 v[32:33], v10, s[16:19], s79 offen offset:1024 sc1\n"
    "s_sub_u32 s69, s74, 49152\n"
    "v_lshrrev_b32 v15, 1, v4\n"
    "v_add_u32 v15, s69, v15\n"
    "ds_read2st64_b64 v[40:43], v15 offset0:0 offset1:1\n"
    "ds_read2st64_b64 v[44:47], v15 offset0:2 offset1:3\n"
    "ds_read2st64_b64 v[48:51], v15 offset0:4 offset1:5\n"
    "ds_read2st64_b64 v[52:55], v15 offset0:6 offset1:7\n"
    "ds_read2st64_b64 v[56:59], v15 offset0:32 offset1:33\n"
    "ds_read2st64_b64 v[60:63], v15 offset0:34 offset1:35\n"
    "s_cmp_eq_u32 s41, 0\n"
    "s_cbranch_scc1 .Lpre1\n"
    ".Lpre1_r:\n"
    "s_lshl_b32 s64, s28, 1\n"
    "s_add_u32 s64, s64, 2\n"
    "s_bitcmp1_b32 s66, 0\n"
    "s_cselect_b32 s64, 0, s64\n"
    "s_cselect_b32 s80, s28, 0\n"
    "s_lshr_b32 s65, s66, 1\n"
    "s_add_u32 s64, s64, s65\n"
    "s_lshl_b32 s64, s64, 4\n"
    "s_add_u32 s64, s64, s30\n"
    "s_mul_i32 s64, s64, 2304\n"
    "s_lshl_b32 s65, s67, 8\n"
    "s_add_u32 s64, s64, s65\n"
    "s_add_u32 s64, s64, 1462272\n"
    "s_add_u32 s69, s42, 15360\n"
    "s_add_u32 s68, s68, s69\n"
    ".Lpp:\n"
    "v_max_u32 v38, v32, v33\n"
    "v_max_f32_dpp v13, v32, v32 quad_perm:[1,0,3,2] row_mask:0xf bank_mask:0xf\n"
    "v_max3_u32 v34, v16, v17, v18\n"
    "v_max_u32 v34, v34, v19\n"
    "v_max_f32_dpp v13, v13, v13 quad_perm:[2,3,0,1] row_mask:0xf bank_mask:0xf\n"
    "v_max3_u32 v35, v20, v21, v22\n"
    "v_max_u32 v35, v35, v23\n"
    "v_max_f32_dpp v13, v13, v13 row_half_mirror row_mask:0xf bank_mask:0xf\n"
    "v_max3_u32 v36, v24, v25, v26\n"
    "v_max_u32 v36, v36, v27\n"
    "v_max_f32_dpp v13, v13, v13 row_mirror row_mask:0xf bank_mask:0xf\n"
    "v_sub_f32 v8, v32, v13\n"
    "v_exp_f32 v8, v8\n"
    "v_max3_u32 v37, v28, v29, v30\n"
    "v_max_u32 v37, v37, v31\n"
    "v_max3_u32 v39, v34, v35, v38\n"
    "v_max3_u32 v39, v39, v36, v37\n"
    "v_cmp_eq_u32 vcc, -1, v39\n"
    "v_mul_f32 v9, v8, v33\n"
    "v_mov_b32_dpp v6, v8 row_newbcast:0 row_mask:0xf bank_mask:0xf\n"
    "v_mov_b32_dpp v7, v8 row_newbcast:4 row_mask:0xf bank_mask:0xf\n"
    "s_cbranch_vccnz .Lpp_re\n"
    ".Lpp_ok:\n"
    "v_add_f32_dpp v9, v9, v9 quad_perm:[1,0,3,2] row_mask:0xf bank_mask:0xf\n"
    "v_pk_mul_f32 v[34:35], v[16:17], v[6:7] op_sel:[0,0] op_sel_hi:[1,0]\n"
    "v_pk_mul_f32 v[36:37], v[18:19], v[6:7] op_sel:[0,0] op_sel_hi:[1,0]\n"
    "v_add_f32_dpp v9, v9, v9 quad_perm:[2,3,0,1] row_mask:0xf bank_mask:0xf\n"
    "v_mov_b32_dpp v6, v8 row_newbcast:8 row_mask:0xf bank_mask:0xf\n"
    "v_pk_fma_f32 v[34:35], v[20:21], v[6:7], v[34:35] op_sel:[0,1,0] op_sel_hi:[1,1,1]\n"
    "v_pk_fma_f32 v[36:37], v[22:23], v[6:7], v[36:37] op_sel:[0,1,0] op_sel_hi:[1,1,1]\n"
    "v_add_f32_dpp v9, v9, v9 row_half_mirror row_mask:0xf bank_mask:0xf\n"
    "v_mov_b32_dpp v7, v8 row_newbcast:12 row_mask:0xf bank_mask:0xf\n"
    "v_pk_fma_f32 v[34:35], v[24:25], v[6:7], v[34:35] op_sel:[0,0,0] op_sel_hi:[1,0,1]\n"
    "v_pk_fma_f32 v[36:37], v[26:27], v[6:7], v[36:37] op_sel:[0,0,0] op_sel_hi:[1,0,1]\n"
    "v_add_f32_dpp v9, v9, v9 row_mirror row_mask:0xf bank_mask:0xf\n"
    "v_pk_fma_f32 v[34:35], v[28:29], v[6:7], v[34:35] op_sel:[0,1,0] op_sel_hi:[1,1,1]\n"
    "v_pk_fma_f32 v[36:37], v[30:31], v[6:7], v[36:37] op_sel:[0,1,0] op_sel_hi:[1,1,1]\n"
    "v_rcp_f32 v9, v9\n"
    "v_mov_b64 v[10:11], 0\n"
    "v_permlane32_swap_b32 v34, v36\n"
    "v_permlane32_swap_b32 v35, v37\n"
    "v_pk_add_f32 v[34:35], v[34:35], v[36:37]\n"
    "v_add_u32 v38, s68, v12\n"
    "v_mov_b64 v[12:13], 0\n"
    "v_permlane16_swap_b32 v34, v35\n"
    "v_add_f32 v34, v34, v35\n"
    "v_mul_f32 v34, v34, v9\n"
    "v_cvt_pk_bf16_f32 v34, v34, 0\n"
    "v_mov_b32 v35, v38\n"
    "s_waitcnt lgkmcnt(0)\n"
    "v_mov_b32_dpp v36, v34 row_newbcast:0 row_mask:0xf bank_mask:0xf\n"
    "v_mov_b32_dpp v37, v34 row_newbcast:1 row_mask:0xf bank_mask:0xf\n"
    "v_mov_b32_dpp v38, v34 row_newbcast:2 row_mask:0xf bank_mask:0xf\n"
    "v_mov_b32_dpp v39, v34 row_newbcast:3 row_mask:0xf bank_mask:0xf\n"
    "v_mov_b32_dpp v6, v34 row_newbcast:4 row_mask:0xf bank_mask:0xf\n"
    "v_mov_b32_dpp v7, v34 row_newbcast:5 row_mask:0xf bank_mask:0xf\n"
    "v_smfmac_f32_16x16x64_bf16 v[10:13], v[36:39], v[40:47], v5\n"
    "ds_read2st64_b64 v[40:43], v15 offset0:36 offset1:37\n"
    "ds_read2st64_b64 v[44:47], v15 offset0:38 offset1:39\n"
    "v_mov_b32_dpp v8, v34 row_newbcast:6 row_mask:0xf bank_mask:0xf\n"
    "v_mov_b32_dpp v9, v34 row_newbcast:7 row_mask:0xf bank_mask:0xf\n"
    "v_mov_b32_dpp v36, v34 row_newbcast:8 row_mask:0xf bank_mask:0xf\n"
    "v_mov_b32_dpp v37, v34 row_newbcast:9 row_mask:0xf bank_mask:0xf\n"
    "v_smfmac_f32_16x16x64_bf16 v[10:13], v[6:9], v[48:55], v5\n"
    "v_mov_b32_dpp v38, v34 row_newbcast:10 row_mask:0xf bank_mask:0xf\n"
    "v_mov_b32_dpp v39, v34 row_newbcast:11 row_mask:0xf bank_mask:0xf\n"
    "v_mov_b32_dpp v6, v34 row_newbcast:12 row_mask:0xf bank_mask:0xf\n"
    "v_mov_b32_dpp v7, v34 row_newbcast:13 row_mask:0xf bank_mask:0xf\n"
    "v_smfmac_f32_16x16x64_bf16 v[10:13], v[36:39], v[56:63], v5\n"
    "v_mov_b32_dpp v8, v34 row_newbcast:14 row_mask:0xf bank_mask:0xf\n"
    "v_mov_b32_dpp v9, v34 row_newbcast:15 row_mask:0xf bank_mask:0xf\n"
    "v_add_u32 v15, s69, v14\n"
    "s_waitcnt lgkmcnt(0)\n"
    "v_smfmac_f32_16x16x64_bf16 v[10:13], v[6:9], v[40:47], v5\n"
    "s_cmp_lg_u32 s28, 0\n"
    "s_nop 6\n"
    "ds_write_b128 v35, v[10:13]\n"
    "s_waitcnt lgkmcnt(0)\n"
    "s_barrier\n"
    "s_cbranch_scc1 .Lwo_x\n"
    "ds_read2st64_b32 v[44:45], v15 offset1:1\n"
    "ds_read2st64_b32 v[46:47], v15 offset0:2 offset1:3\n"
    "s_waitcnt lgkmcnt(0)\n"
    "v_pk_add_f32 v[44:45], v[44:45], v[46:47]\n"
    "v_add_f32 v44, v44, v45\n"
    "v_min_u32 v44, -2, v44\n"
    "buffer_store_dword v44, v14, s[16:19], s64 offen sc1\n"
    ".Lwo_x:\n"
    "s_cmp_eq_u32 s41, 2\n"
    "s_cbranch_scc1 .Lwo2\n"
    ".Lnotask:\n"
    "s_lshr_b32 s51, s41, 1\n"
    "s_bfe_u32 s64, s29, 0\n"
    "s_lshl_b32 s64, s64, 1\n"
    "s_add_u32 s64, s64, 2\n"
    "s_add_u32 s64, s64, s51\n"
    "s_cmp_eq_u32 s41, 0\n"
    "s_cbranch_scc1 .Lpre2\n"
    "s_cmp_eq_u32 s41, 3\n"
    "s_cbranch_scc1 .Lfin\n"
    ".Lh2:\n"
    "s_mul_i32 s74, s64, 36864\n"
    "s_lshl_b32 s65, s28, 6\n"
    "s_add_u32 s74, s74, 1462272\n"
    "s_add_u32 s82, s74, s65\n"
    "s_lshl_b32 s65, s28, 9\n"
    "s_add_u32 s74, s74, s65\n"
    "v_lshrrev_b32 v10, 9, v4\n"
    "v_and_b32 v11, 0x1f0, v4\n"
    "v_mul_u32_u24 v8, 2304, v10\n"
    "v_add_u32 v8, v8, v11\n"
    "v_lshrrev_b32 v9, 6, v4\n"
    "v_mul_u32_u24 v9, 2304, v9\n"
    "v_and_b32 v12, 48, v4\n"
    "v_add_u32 v9, v9, v12\n"
    "s_add_u32 s75, s74, 4608\n"
    "s_add_u32 s76, s74, 9216\n"
    "s_add_u32 s77, s74, 13824\n"
    "s_add_u32 s78, s74, 18432\n"
    "s_add_u32 s79, s74, 23040\n"
    "s_add_u32 s80, s74, 27648\n"
    "s_add_u32 s81, s74, 32256\n"
    "buffer_load_dwordx4 v[48:51], v9, s[16:19], s82 offen offset:2048 sc1\n"
    "buffer_load_dwordx4 v[16:19], v8, s[16:19], s74 offen sc1\n"
    "buffer_load_dwordx4 v[20:23], v8, s[16:19], s75 offen sc1\n"
    "buffer_load_dwordx4 v[24:27], v8, s[16:19], s76 offen sc1\n"
    "buffer_load_dwordx4 v[28:31], v8, s[16:19], s77 offen sc1\n"
    "buffer_load_dwordx4 v[32:35], v8, s[16:19], s78 offen sc1\n"
    "buffer_load_dwordx4 v[36:39], v8, s[16:19], s79 offen sc1\n"
    "buffer_load_dwordx4 v[40:43], v8, s[16:19], s80 offen sc1\n"
    "buffer_load_dwordx4 v[44:47], v8, s[16:19], s81 offen sc1\n"
    "s_add_u32 s69, s42, 15360\n"
    "s_mul_i32 s65, s51, 1152\n"
    "s_add_u32 s65, s65, 95232\n"
    "s_lshr_b32 s66, s31, 2\n"
    "v_add_u32 v7, s65, v4\n"
    "v_lshrrev_b32 v15, 6, v4\n"
    "v_add_u32 v13, s66, v15\n"
    "v_and_b32 v13, 3, v13\n"
    "v_and_b32 v57, 12, v15\n"
    "v_bfe_u32 v58, v4, 4, 2\n"
    "v_or_b32 v57, v57, v58\n"
    "v_lshl_or_b32 v13, v13, 4, v57\n"
    "v_and_b32 v15, 1, v15\n"
    "v_lshl_or_b32 v15, v15, 6, v57\n"
    "v_lshl_add_u32 v57, v13, 1, s65\n"
    "v_and_b32 v56, 0x300, v4\n"
    "v_and_b32 v58, 0x78, v3\n"
    "v_or_b32 v56, v56, v58\n"
    "v_add_u32 v61, 93184, v56\n"
    "s_lshl_b32 s67, s28, 11\n"
    "s_add_u32 s67, s67, 76800\n"
    "v_add_u32 v56, s67, v56\n"
    "s_lshl_b32 s67, s28, 8\n"
    "s_add_u32 s67, s67, s69\n"
    "v_lshl_add_u32 v58, v58, 1, s67\n"
    "s_cmp_eq_u32 s28, 0\n"
    "s_cselect_b32 s67, 0xffff, 0\n"
    "v_mov_b32 v59, s67\n"
    "v_mov_b32 v60, 0xffff0000\n"
    "v_lshl_or_b32 v10, v10, 3, v11\n"
    "v_lshrrev_b32 v11, 8, v4\n"
    "v_lshl_or_b32 v11, v11, 2, v12\n"
    "v_mov_b32 v54, 0\n"
    "s_lshl_b32 s67, s28, 8\n"
    "s_add_u32 s67, s67, s65\n"
    "v_lshrrev_b32 v52, 1, v10\n"
    "v_add_u32 v52, s67, v52\n"
    "ds_read_b32 v53, v52\n"
    "s_lshl_b32 s67, s28, 5\n"
    "s_add_u32 s67, s67, s65\n"
    "v_lshrrev_b32 v52, 1, v11\n"
    "v_add_u32 v52, s67, v52\n"
    "ds_read_u16_d16_hi v54, v52 offset:1024\n"
    "s_add_u32 s68, s42, 3072\n"
    "v_mov_b32 v62, s68\n"
    "v_lshl_add_u32 v13, v13, 2, s68\n"
    "s_lshl_b32 s67, s28, 6\n"
    "s_add_u32 s67, s67, s68\n"
    "v_add_u32 v11, s67, v11\n"
    "s_lshr_b32 s65, s66, 1\n"
    "s_lshl_b32 s65, s65, 12\n"
    "s_add_u32 s67, s67, s65\n"
    "s_and_b32 s65, s66, 1\n"
    "s_lshl_b32 s65, s65, 9\n"
    "s_add_u32 s67, s67, s65\n"
    "v_lshl_add_u32 v15, v15, 2, s67\n"
    "s_lshr_b32 s65, s28, 1\n"
    "s_lshl_b32 s65, s65, 12\n"
    "s_add_u32 s68, s68, s65\n"
    "s_and_b32 s65, s28, 1\n"
    "s_lshl_b32 s65, s65, 9\n"
    "s_add_u32 s68, s68, s65\n"
    "v_add_u32 v10, s68, v10\n"
    "v_and_b32 v12, 0x200, v4\n"
    "v_mul_u32_u24 v12, 6, v12\n"
    "v_lshl_add_u32 v12, v4, 1, v12\n"
    "v_add_u32 v12, v12, v62\n"
    "v_add_u32 v63, s69, v14\n"
    "s_add_u32 s50, s41, 1\n"
    "s_lshl_b32 s50, s50, 4\n"
    "s_add_u32 s50, s50, s30\n"
    "s_lshl_b32 s50, s50, 12\n"
    "s_add_u32 s50, s50, 1183744\n"
    "s_lshl_b32 s70, s31, 8\n"
    "s_add_u32 s70, s70, s50\n"
    "s_lshl_b32 s71, s41, 4\n"
    "s_add_u32 s71, s71, s31\n"
    "s_waitcnt lgkmcnt(0)\n"
    "v_lshlrev_b32 v52, 16, v53\n"
    "v_and_b32 v53, v60, v53\n"
    ".Lyp:\n"
    "s_waitcnt vmcnt(8)\n"
    "v_max3_u32 v6, v48, v49, v50\n"
    "v_max_u32 v6, v6, v51\n"
    "v_permlane32_swap_b32 v48, v50\n"
    "v_permlane32_swap_b32 v49, v51\n"
    "v_pk_add_f32 v[48:49], v[48:49], v[50:51]\n"
    "s_waitcnt vmcnt(6)\n"
    "v_max3_u32 v6, v6, v16, v17\n"
    "v_max3_u32 v6, v6, v18, v19\n"
    "v_permlane16_swap_b32 v48, v49\n"
    "v_add_f32 v48, v48, v49\n"
    "v_max3_u32 v6, v6, v20, v21\n"
    "v_max3_u32 v6, v6, v22, v23\n"
    "v_add_f32_dpp v48, v48, v48 row_ror:4 row_mask:0xf bank_mask:0xf\n"
    "v_pk_add_f32 v[16:17], v[16:17], v[20:21]\n"
    "v_pk_add_f32 v[18:19], v[18:19], v[22:23]\n"
    "v_add_f32_dpp v48, v48, v48 row_ror:8 row_mask:0xf bank_mask:0xf\n"
    "v_cvt_pk_bf16_f32 v48, 0, v48\n"
    "v_add_f32 v48, v48, v54\n"
    "v_cvt_pk_bf16_f32 v48, 0, v48\n"
    "ds_write_b32 v11, v48 offset:8192\n"
    "s_waitcnt vmcnt(5)\n"
    "v_max3_u32 v6, v6, v24, v25\n"
    "v_max3_u32 v6, v6, v26, v27\n"
    "v_pk_add_f32 v[16:17], v[16:17], v[24:25]\n"
    "v_pk_add_f32 v[18:19], v[18:19], v[26:27]\n"
    "s_waitcnt vmcnt(4)\n"
    "v_max3_u32 v6, v6, v28, v29\n"
    "v_max3_u32 v6, v6, v30, v31\n"
    "v_pk_add_f32 v[16:17], v[16:17], v[28:29]\n"
    "v_pk_add_f32 v[18:19], v[18:19], v[30:31]\n"
    "s_waitcnt vmcnt(3)\n"
    "v_max3_u32 v6, v6, v32, v33\n"
    "v_max3_u32 v6, v6, v34, v35\n"
    "v_pk_add_f32 v[16:17], v[16:17], v[32:33]\n"
    "v_pk_add_f32 v[18:19], v[18:19], v[34:35]\n"
    "s_waitcnt vmcnt(2)\n"
    "v_max3_u32 v6, v6, v36, v37\n"
    "v_max3_u32 v6, v6, v38, v39\n"
    "v_pk_add_f32 v[16:17], v[16:17], v[36:37]\n"
    "v_pk_add_f32 v[18:19], v[18:19], v[38:39]\n"
    "s_waitcnt vmcnt(1)\n"
    "v_max3_u32 v6, v6, v40, v41\n"
    "v_max3_u32 v6, v6, v42, v43\n"
    "v_pk_add_f32 v[16:17], v[16:17], v[40:41]\n"
    "v_pk_add_f32 v[18:19], v[18:19], v[42:43]\n"
    "s_waitcnt vmcnt(0)\n"
    "v_pk_add_f32 v[16:17], v[16:17], v[44:45]\n"
    "v_pk_add_f32 v[18:19], v[18:19], v[46:47]\n"
    "v_max3_u32 v6, v6, v44, v45\n"
    "v_max3_u32 v6, v6, v46, v47\n"
    "v_permlane32_swap_b32 v16, v18\n"
    "v_permlane32_swap_b32 v17, v19\n"
    "v_cmp_eq_u32 vcc, -1, v6\n"
    "v_pk_add_f32 v[16:17], v[16:17], v[18:19]\n"
    "v_cvt_pk_bf16_f32 v16, 0, v16\n"
    "v_cvt_pk_bf16_f32 v17, 0, v17\n"
    "v_pk_add_f32 v[16:17], v[16:17], v[52:53]\n"
    "v_cvt_pk_bf16_f32 v16, 0, v16\n"
    "v_cvt_pk_bf16_f32 v17, 0, v17\n"
    "ds_write_b64 v10, v[16:17]\n"
    "s_cbranch_vccnz .Lyp_re\n"
    ".Lyp_ok:\n"
    "s_cmp_lt_u32 s71, 9\n"
    "s_cbranch_scc1 .Lp2\n"
    ".Lp2_r:\n"
    "s_waitcnt lgkmcnt(0)\n"
    "s_barrier\n"
    "ds_read_b32 v62, v62\n"
    "ds_read_b128 v[16:19], v12\n"
    "ds_read_b128 v[20:23], v12 offset:16\n"
    "ds_read_b32 v24, v13 offset:8192\n"
    "ds_read_b32 v25, v15\n"
    "s_waitcnt lgkmcnt(3)\n"
    "v_pk_add_f32 v[28:29], v[16:17], v[62:63] op_sel_hi:[1,0] neg_lo:[0,1] neg_hi:[0,1]\n"
    "v_pk_add_f32 v[30:31], v[18:19], v[62:63] op_sel_hi:[1,0] neg_lo:[0,1] neg_hi:[0,1]\n"
    "v_pk_mul_f32 v[38:39], v[28:29], v[28:29]\n"
    "v_pk_add_f32 v[36:37], v[28:29], v[30:31]\n"
    "v_pk_fma_f32 v[38:39], v[30:31], v[30:31], v[38:39]\n"
    "s_waitcnt lgkmcnt(2)\n"
    "v_pk_add_f32 v[32:33], v[20:21], v[62:63] op_sel_hi:[1,0] neg_lo:[0,1] neg_hi:[0,1]\n"
    "v_pk_add_f32 v[36:37], v[36:37], v[32:33]\n"
    "v_pk_fma_f32 v[38:39], v[32:33], v[32:33], v[38:39]\n"
    "v_pk_add_f32 v[34:35], v[22:23], v[62:63] op_sel_hi:[1,0] neg_lo:[0,1] neg_hi:[0,1]\n"
    "v_pk_add_f32 v[36:37], v[36:37], v[34:35]\n"
    "v_pk_fma_f32 v[38:39], v[34:35], v[34:35], v[38:39]\n"
    "s_waitcnt lgkmcnt(0)\n"
    "v_pk_add_f32 v[26:27], v[24:25], v[62:63] op_sel_hi:[1,0] neg_lo:[0,1] neg_hi:[0,1]\n"
    "v_add_f32 v8, v36, v37\n"
    "v_add_f32 v9, v38, v39\n"
    "v_add_f32 v8, v8, v26\n"
    "v_fmac_f32 v9, v26, v26\n"
    "v_mov_b64 v[40:41], 0\n"
    "v_add_f32_dpp v8, v8, v8 quad_perm:[1,0,3,2] row_mask:0xf bank_mask:0xf\n"
    "v_add_f32_dpp v9, v9, v9 quad_perm:[1,0,3,2] row_mask:0xf bank_mask:0xf\n"
    "v_mov_b64 v[42:43], 0\n"
    "v_add_f32_dpp v8, v8, v8 quad_perm:[2,3,0,1] row_mask:0xf bank_mask:0xf\n"
    "v_add_f32_dpp v9, v9, v9 quad_perm:[2,3,0,1] row_mask:0xf bank_mask:0xf\n"
    "v_mov_b64 v[44:45], 0\n"
    "v_add_f32_dpp v8, v8, v8 row_half_mirror row_mask:0xf bank_mask:0xf\n"
    "v_add_f32_dpp v9, v9, v9 row_half_mirror row_mask:0xf bank_mask:0xf\n"
    "v_mov_b64 v[46:47], 0\n"
    "v_add_f32_dpp v8, v8, v8 row_mirror row_mask:0xf bank_mask:0xf\n"
    "v_add_f32_dpp v9, v9, v9 row_mirror row_mask:0xf bank_mask:0xf\n"
    "v_mov_b64 v[48:49], 0\n"
    "v_add_f32_dpp v8, v8, v8 row_bcast:15 row_mask:0xa bank_mask:0xf\n"
    "v_add_f32_dpp v9, v9, v9 row_bcast:15 row_mask:0xa bank_mask:0xf\n"
    "s_nop 0\n"
    "v_add_f32_dpp v8, v8, v8 row_bcast:31 row_mask:0xc bank_mask:0xf\n"
    "v_add_f32_dpp v9, v9, v9 row_bcast:31 row_mask:0xc bank_mask:0xf\n"
    "v_pk_mul_f32 v[8:9], v[8:9], s[98:99] op_sel_hi:[1,0]\n"
    "v_fma_f32 v9, -v8, v8, v9\n"
    "v_cvt_pk_bf16_f32 v10, 0, v9\n"
    "v_add_f32 v10, 0x3727c5ac, v10\n"
    "v_cvt_pk_bf16_f32 v10, 0, v10\n"
    "v_sqrt_f32 v10, v10\n"
    "v_add_f32 v12, v62, v8\n"
    "v_cvt_pk_bf16_f32 v10, 0, v10\n"
    "v_rcp_f32 v10, v10\n"
    "v_cvt_pk_bf16_f32 v12, 0, v12\n"
    "v_mul_f32 v9, v9, v10\n"
    "v_mul_f32 v9, v9, v10\n"
    "v_cvt_pk_bf16_f32 v13, 0, v9\n"
    "v_add_f32 v13, 0x3727c5ac, v13\n"
    "v_cvt_pk_bf16_f32 v13, 0, v13\n"
    "v_sqrt_f32 v13, v13\n"
    "v_sub_f32 v12, v62, v12\n"
    "v_cvt_pk_bf16_f32 v13, 0, v13\n"
    "v_rcp_f32 v13, v13\n"
    "v_add_f32 v8, v8, v12\n"
    "v_mul_f32 v8, v8, v10\n"
    "v_cvt_pk_bf16_f32 v8, 0, v8\n"
    "v_fma_f32 v11, v12, v10, -v8\n"
    "v_mul_f32 v13, 0x3c800000, v13\n"
    "v_pk_mul_f32 v[10:11], v[10:11], v[12:13] op_sel:[0,1]\n"
    "s_nop 0\n"
    "v_readlane_b32 s64, v10, 63\n"
    "v_readlane_b32 s65, v11, 63\n"
    "s_lshl_b32 s66, s64, 1\n"
    "s_cmp_gt_u32 s66, 0xff000000\n"
    "v_pk_fma_f32 v[28:29], v[28:29], s[64:65], s[64:65] op_sel:[0,0,1] op_sel_hi:[1,0,1]\n"
    "v_cvt_pk_bf16_f32 v28, v28, v28\n"
    "v_bfe_u32 v28, v28, 14, 17\n"
    "ds_read_u16_d16_hi v40, v28 offset:12288\n"
    "v_cvt_pk_bf16_f32 v29, v29, v29\n"
    "v_bfe_u32 v29, v29, 14, 17\n"
    "ds_read_u16_d16_hi v41, v29 offset:12288\n"
    "v_pk_fma_f32 v[30:31], v[30:31], s[64:65], s[64:65] op_sel:[0,0,1] op_sel_hi:[1,0,1]\n"
    "v_cvt_pk_bf16_f32 v30, v30, v30\n"
    "v_bfe_u32 v30, v30, 14, 17\n"
    "ds_read_u16_d16_hi v42, v30 offset:12288\n"
    "v_cvt_pk_bf16_f32 v31, v31, v31\n"
    "v_bfe_u32 v31, v31, 14, 17\n"
    "ds_read_u16_d16_hi v43, v31 offset:12288\n"
    "v_pk_fma_f32 v[32:33], v[32:33], s[64:65], s[64:65] op_sel:[0,0,1] op_sel_hi:[1,0,1]\n"
    "v_cvt_pk_bf16_f32 v32, v32, v32\n"
    "v_bfe_u32 v32, v32, 14, 17\n"
    "ds_read_u16_d16_hi v44, v32 offset:12288\n"
    "v_cvt_pk_bf16_f32 v33, v33, v33\n"
    "v_bfe_u32 v33, v33, 14, 17\n"
    "ds_read_u16_d16_hi v45, v33 offset:12288\n"
    "v_pk_fma_f32 v[34:35], v[34:35], s[64:65], s[64:65] op_sel:[0,0,1] op_sel_hi:[1,0,1]\n"
    "v_cvt_pk_bf16_f32 v34, v34, v34\n"
    "v_bfe_u32 v34, v34, 14, 17\n"
    "ds_read_u16_d16_hi v46, v34 offset:12288\n"
    "v_cvt_pk_bf16_f32 v35, v35, v35\n"
    "v_bfe_u32 v35, v35, 14, 17\n"
    "ds_read_u16_d16_hi v47, v35 offset:12288\n"
    "v_pk_fma_f32 v[26:27], v[26:27], s[64:65], s[64:65] op_sel:[0,0,1] op_sel_hi:[1,0,1]\n"
    "v_cvt_pk_bf16_f32 v26, v26, v26\n"
    "v_bfe_u32 v26, v26, 14, 17\n"
    "ds_read_u16_d16_hi v48, v26 offset:12288\n"
    "v_cvt_pk_bf16_f32 v27, v27, v27\n"
    "v_bfe_u32 v27, v27, 14, 17\n"
    "ds_read_u16_d16_hi v49, v27 offset:12288\n"
    "s_cselect_b32 s66, -1, 53248\n"
    "v_min3_u32 v8, v28, v29, v30\n"
    "v_min3_u32 v9, v31, v32, v33\n"
    "v_min3_u32 v8, v8, v34, v35\n"
    "v_min3_u32 v9, v9, v26, v27\n"
    "v_min_u32 v8, v8, v9\n"
    "v_cmp_gt_u32 vcc, s66, v8\n"
    "s_cmp_eq_u32 s41, 3\n"
    "s_waitcnt lgkmcnt(6)\n"
    "v_pk_add_f32 v[36:37], v[16:17], v[40:41]\n"
    "v_pk_add_f32 v[38:39], v[18:19], v[42:43]\n"
    "s_cbranch_vccnz .Lfb\n"
    ".Lfb_r:\n"
    "v_cvt_pk_bf16_f32 v16, 0, v36\n"
    "s_waitcnt lgkmcnt(0)\n"
    "v_readfirstlane_b32 s64, v16\n"
    "v_pk_add_f32 v[20:21], v[20:21], v[44:45]\n"
    "v_pk_add_f32 v[22:23], v[22:23], v[46:47]\n"
    "v_pk_add_f32 v[24:25], v[24:25], v[48:49]\n"
    "s_cbranch_scc1 .Lout\n"
    "v_cvt_pk_bf16_f32 v17, 0, v37\n"
    "v_cvt_pk_bf16_f32 v18, 0, v38\n"
    "v_cvt_pk_bf16_f32 v19, 0, v39\n"
    "v_cvt_pk_bf16_f32 v20, 0, v20\n"
    "v_cvt_pk_bf16_f32 v21, 0, v21\n"
    "v_cvt_pk_bf16_f32 v22, 0, v22\n"
    "v_cvt_pk_bf16_f32 v23, 0, v23\n"
    "v_cvt_pk_bf16_f32 v24, 0, v24\n"
    "v_cvt_pk_bf16_f32 v25, 0, v25\n"
    ".Llnq:\n"
    "v_pk_add_f32 v[28:29], v[16:17], s[64:65] op_sel_hi:[1,0] neg_lo:[0,1] neg_hi:[0,1]\n"
    "v_pk_add_f32 v[30:31], v[18:19], s[64:65] op_sel_hi:[1,0] neg_lo:[0,1] neg_hi:[0,1]\n"
    "v_pk_mul_f32 v[38:39], v[28:29], v[28:29]\n"
    "v_pk_add_f32 v[36:37], v[28:29], v[30:31]\n"
    "v_pk_fma_f32 v[38:39], v[30:31], v[30:31], v[38:39]\n"
    "v_pk_add_f32 v[32:33], v[20:21], s[64:65] op_sel_hi:[1,0] neg_lo:[0,1] neg_hi:[0,1]\n"
    "v_pk_add_f32 v[36:37], v[36:37], v[32:33]\n"
    "v_pk_fma_f32 v[38:39], v[32:33], v[32:33], v[38:39]\n"
    "v_pk_add_f32 v[34:35], v[22:23], s[64:65] op_sel_hi:[1,0] neg_lo:[0,1] neg_hi:[0,1]\n"
    "v_pk_add_f32 v[36:37], v[36:37], v[34:35]\n"
    "v_pk_fma_f32 v[38:39], v[34:35], v[34:35], v[38:39]\n"
    "v_pk_add_f32 v[26:27], v[24:25], s[64:65] op_sel_hi:[1,0] neg_lo:[0,1] neg_hi:[0,1]\n"
    "v_add_f32 v8, v36, v37\n"
    "v_add_f32 v9, v38, v39\n"
    "v_add_f32 v8, v8, v26\n"
    "v_fmac_f32 v9, v26, v26\n"
    "v_add_u32 v6, 8192, v56\n"
    "v_add_f32_dpp v8, v8, v8 quad_perm:[1,0,3,2] row_mask:0xf bank_mask:0xf\n"
    "v_add_f32_dpp v9, v9, v9 quad_perm:[1,0,3,2] row_mask:0xf bank_mask:0xf\n"
    "ds_read2_b64 v[48:51], v61 offset0:0 offset1:16\n"
    "v_add_f32_dpp v8, v8, v8 quad_perm:[2,3,0,1] row_mask:0xf bank_mask:0xf\n"
    "v_add_f32_dpp v9, v9, v9 quad_perm:[2,3,0,1] row_mask:0xf bank_mask:0xf\n"
    "ds_read2_b64 v[52:55], v61 offset0:128 offset1:144\n"
    "v_add_f32_dpp v8, v8, v8 row_half_mirror row_mask:0xf bank_mask:0xf\n"
    "v_add_f32_dpp v9, v9, v9 row_half_mirror row_mask:0xf bank_mask:0xf\n"
    "ds_read2_b64 v[40:43], v6 offset0:0 offset1:16\n"
    "v_add_f32_dpp v8, v8, v8 row_mirror row_mask:0xf bank_mask:0xf\n"
    "v_add_f32_dpp v9, v9, v9 row_mirror row_mask:0xf bank_mask:0xf\n"
    "ds_read2_b64 v[44:47], v6 offset0:128 offset1:144\n"
    "v_add_f32_dpp v8, v8, v8 row_bcast:15 row_mask:0xa bank_mask:0xf\n"
    "v_add_f32_dpp v9, v9, v9 row_bcast:15 row_mask:0xa bank_mask:0xf\n"
    "ds_read2_b64 v[32:35], v56 offset0:0 offset1:16\n"
    "v_add_f32_dpp v8, v8, v8 row_bcast:31 row_mask:0xc bank_mask:0xf\n"
    "v_add_f32_dpp v9, v9, v9 row_bcast:31 row_mask:0xc bank_mask:0xf\n"
    "ds_read2_b64 v[36:39], v56 offset0:128 offset1:144\n"
    "v_pk_mul_f32 v[8:9], v[8:9], s[98:99] op_sel_hi:[1,0]\n"
    "v_fma_f32 v9, -v8, v8, v9\n"
    "v_cvt_pk_bf16_f32 v10, 0, v9\n"
    "v_add_f32 v10, 0x3727c5ac, v10\n"
    "v_cvt_pk_bf16_f32 v10, 0, v10\n"
    "v_sqrt_f32 v10, v10\n"
    "v_add_f32 v12, s64, v8\n"
    "v_cvt_pk_bf16_f32 v10, 0, v10\n"
    "v_rcp_f32 v10, v10\n"
    "v_cvt_pk_bf16_f32 v12, 0, v12\n"
    "v_sub_f32 v12, s64, v12\n"
    "v_readlane_b32 s64, v10, 63\n"
    "v_mul_f32 v11, v12, v10\n"
    "v_cvt_pk_bf16_f32 v28, v16, v17\n"
    "v_readlane_b32 s65, v11, 63\n"
    "v_mov_b64 v[10:11], 0\n"
    "v_mov_b64 v[12:13], 0\n"
    "v_pk_fma_f32 v[26:27], v[26:27], s[64:65], s[64:65] op_sel:[0,0,1] op_sel_hi:[1,0,1]\n"
    "v_cvt_pk_bf16_f32 v26, v26, v27\n"
    "v_cvt_pk_bf16_f32 v29, v18, v19\n"
    "v_cvt_pk_bf16_f32 v30, v20, v21\n"
    "v_cvt_pk_bf16_f32 v31, v22, v23\n"
    "ds_write_b128 v7, v[28:31]\n"
    "ds_write_b16_d16_hi v57, v24 offset:1024\n"
    "v_and_b32_dpp v16, v26, v60 row_newbcast:0 row_mask:0xf bank_mask:0xf\n"
    "v_and_b32_dpp v17, v26, v60 row_newbcast:1 row_mask:0xf bank_mask:0xf\n"
    "v_and_b32_dpp v18, v26, v60 row_newbcast:2 row_mask:0xf bank_mask:0xf\n"
    "v_and_b32_dpp v19, v26, v60 row_newbcast:3 row_mask:0xf bank_mask:0xf\n"
    "v_and_b32_dpp v20, v26, v60 row_newbcast:4 row_mask:0xf bank_mask:0xf\n"
    "v_and_b32_dpp v21, v26, v60 row_newbcast:5 row_mask:0xf bank_mask:0xf\n"
    "s_waitcnt lgkmcnt(2)\n"
    "v_smfmac_f32_16x16x64_bf16 v[10:13], v[16:19], v[32:39], v5\n"
    "v_and_b32_dpp v22, v26, v60 row_newbcast:6 row_mask:0xf bank_mask:0xf\n"
    "v_and_b32_dpp v23, v26, v60 row_newbcast:7 row_mask:0xf bank_mask:0xf\n"
    "v_and_b32_dpp v16, v26, v59 row_newbcast:0 row_mask:0xf bank_mask:0xf\n"
    "v_and_b32_dpp v17, v26, v59 row_newbcast:1 row_mask:0xf bank_mask:0xf\n"
    "v_smfmac_f32_16x16x64_bf16 v[10:13], v[20:23], v[40:47], v5\n"
    "v_and_b32_dpp v18, v26, v59 row_newbcast:2 row_mask:0xf bank_mask:0xf\n"
    "v_and_b32_dpp v19, v26, v59 row_newbcast:3 row_mask:0xf bank_mask:0xf\n"
    "s_nop 1\n"
    "v_smfmac_f32_16x16x64_bf16 v[10:13], v[16:19], v[48:55], v5\n"
    "s_cmp_lg_u32 s45, 0\n"
    "s_nop 6\n"
    "ds_write_b128 v58, v[10:13]\n"
    "s_cbranch_scc1 .Llnq_ret\n"
    "s_add_u32 s41, s41, 1\n"
    "s_cmp_lg_u32 s28, 0\n"
    "s_waitcnt lgkmcnt(0)\n"
    "s_barrier\n"
    "s_cbranch_scc1 .Lloop\n"
    "ds_read2st64_b32 v[44:45], v63 offset1:1\n"
    "ds_read2st64_b32 v[46:47], v63 offset0:2 offset1:3\n"
    "s_waitcnt lgkmcnt(0)\n"
    "v_pk_add_f32 v[44:45], v[44:45], v[46:47]\n"
    "v_add_f32 v44, v44, v45\n"
    "v_min_u32 v44, -2, v44\n"
    "buffer_store_dword v44, v14, s[16:19], s70 offen sc1\n"
    "s_branch .Lloop\n"
    ".Lout:\n"
    "s_cmp_lg_u32 s28, 0\n"
    "v_cvt_pk_bf16_f32 v16, v36, v37\n"
    "v_cvt_pk_bf16_f32 v17, v38, v39\n"
    "v_cvt_pk_bf16_f32 v18, v20, v21\n"
    "v_cvt_pk_bf16_f32 v19, v22, v23\n"
    "s_cbranch_scc1 .Lend\n"
    "v_subrev_u32 v7, 95232, v7\n"
    "v_cvt_pk_bf16_f32 v24, 0, v24\n"
    "v_subrev_u32 v57, 95232, v57\n"
    "global_store_dwordx4 v7, v[16:19], s[62:63]\n"
    "global_store_short_d16_hi v57, v24, s[62:63] offset:1024\n"
    "s_branch .Lend\n"
    ".Llnq0:\n"
    "s_lshl_b32 s69, s51, 12\n"
    "s_add_u32 s69, s69, 150528\n"
    "s_mul_i32 s65, s51, 1152\n"
    "s_add_u32 s65, s65, 95232\n"
    "s_lshr_b32 s66, s31, 2\n"
    "v_add_u32 v7, s65, v4\n"
    "v_lshrrev_b32 v15, 6, v4\n"
    "v_add_u32 v13, s66, v15\n"
    "v_and_b32 v13, 3, v13\n"
    "v_and_b32 v57, 12, v15\n"
    "v_bfe_u32 v58, v4, 4, 2\n"
    "v_or_b32 v57, v57, v58\n"
    "v_lshl_or_b32 v13, v13, 4, v57\n"
    "v_and_b32 v15, 1, v15\n"
    "v_lshl_or_b32 v15, v15, 6, v57\n"
    "v_lshl_add_u32 v57, v13, 1, s65\n"
    "v_and_b32 v56, 0x300, v4\n"
    "v_and_b32 v58, 0x78, v3\n"
    "v_or_b32 v56, v56, v58\n"
    "v_add_u32 v61, 93184, v56\n"
    "s_lshl_b32 s67, s28, 11\n"
    "s_add_u32 s67, s67, 76800\n"
    "v_add_u32 v56, s67, v56\n"
    "s_lshl_b32 s67, s28, 8\n"
    "s_add_u32 s67, s67, s69\n"
    "v_lshl_add_u32 v58, v58, 1, s67\n"
    "s_cmp_eq_u32 s28, 0\n"
    "s_cselect_b32 s67, 0xffff, 0\n"
    "v_mov_b32 v59, s67\n"
    "v_mov_b32 v60, 0xffff0000\n"
    "v_mov_b64 v[24:25], 0\n"
    "s_lshl_b32 s66, s66, 8\n"
    "s_add_u32 s65, s65, s66\n"
    "s_lshl_b32 s66, s28, 5\n"
    "s_add_u32 s65, s65, s66\n"
    "v_lshl_add_u32 v15, v15, 1, s65\n"
    "ds_read_b128 v[28:31], v7\n"
    "ds_read_u16_d16_hi v24, v57 offset:1024\n"
    "ds_read_u16_d16_hi v25, v15\n"
    "s_waitcnt lgkmcnt(0)\n"
    "v_lshlrev_b32 v16, 16, v28\n"
    "v_and_b32 v17, v60, v28\n"
    "v_lshlrev_b32 v18, 16, v29\n"
    "v_and_b32 v19, v60, v29\n"
    "v_lshlrev_b32 v20, 16, v30\n"
    "v_and_b32 v21, v60, v30\n"
    "v_lshlrev_b32 v22, 16, v31\n"
    "v_and_b32 v23, v60, v31\n"
    "v_readfirstlane_b32 s64, v16\n"
    "s_nop 1\n"
    "s_branch .Llnq\n"
    ".Llnq_ret:\n"
    "s_setpc_b64 s[44:45]\n"
    ".Ltab:\n"
    "s_waitcnt lgkmcnt(0)\n"
    "v_rcp_f32 v41, v40\n"
    "v_mul_f32 v42, s99, v38\n"
    "v_mul_f32 v43, 0x3eeff558, v39\n"
    "v_add_u32 v8, 512, v8\n"
    "v_mul_f32 v41, 0x3eeff558, v41\n"
    "ds_read_u16_d16_hi v38, v8\n"
    "ds_read_u16_d16_hi v39, v8 offset:9216\n"
    "ds_read_u16_d16_hi v40, v8 offset:18432\n"
    "v_med3_f32 v41, v41, -s53, s53\n"
    "v_pk_fma_f32 v[44:45], v[42:43], s[74:75], v[42:43] op_sel:[0,0,1] op_sel_hi:[0,1,1]\n"
    "v_pk_fma_f32 v[50:51], v[44:45], v[44:45], s[52:53] op_sel_hi:[1,1,0]\n"
    "v_pk_mul_f32 v[50:51], v[50:51], v[44:45] neg_lo:[1,0] neg_hi:[1,0]\n"
    "v_exp_f32 v50, v50\n"
    "v_pk_fma_f32 v[46:47], v[42:43], s[76:77], v[42:43] op_sel:[0,0,1] op_sel_hi:[0,1,1]\n"
    "v_exp_f32 v51, v51\n"
    "v_pk_fma_f32 v[52:53], v[46:47], v[46:47], s[52:53] op_sel_hi:[1,1,0]\n"
    "v_pk_mul_f32 v[52:53], v[52:53], v[46:47] neg_lo:[1,0] neg_hi:[1,0]\n"
    "v_pk_fma_f32 v[50:51], v[50:51], v[40:41], v[40:41] op_sel:[0,1,1] op_sel_hi:[1,1,1]\n"
    "v_exp_f32 v52, v52\n"
    "v_pk_fma_f32 v[48:49], v[42:43], s[78:79], v[42:43] op_sel:[0,0,1] op_sel_hi:[0,1,1]\n"
    "v_exp_f32 v53, v53\n"
    "v_pk_fma_f32 v[54:55], v[48:49], v[48:49], s[52:53] op_sel_hi:[1,1,0]\n"
    "v_rcp_f32 v50, v50\n"
    "v_pk_mul_f32 v[54:55], v[54:55], v[48:49] neg_lo:[1,0] neg_hi:[1,0]\n"
    "v_rcp_f32 v51, v51\n"
    "v_pk_fma_f32 v[52:53], v[52:53], v[40:41], v[40:41] op_sel:[0,1,1] op_sel_hi:[1,1,1]\n"
    "v_pk_fma_f32 v[16:17], v[44:45], v[50:51], v[16:17]\n"
    "v_exp_f32 v54, v54\n"
    "v_pk_fma_f32 v[44:45], v[42:43], s[80:81], v[42:43] op_sel:[0,0,1] op_sel_hi:[0,1,1]\n"
    "v_exp_f32 v55, v55\n"
    "v_pk_fma_f32 v[50:51], v[44:45], v[44:45], s[52:53] op_sel_hi:[1,1,0]\n"
    "v_rcp_f32 v52, v52\n"
    "v_pk_mul_f32 v[50:51], v[50:51], v[44:45] neg_lo:[1,0] neg_hi:[1,0]\n"
    "v_rcp_f32 v53, v53\n"
    "v_pk_fma_f32 v[54:55], v[54:55], v[40:41], v[40:41] op_sel:[0,1,1] op_sel_hi:[1,1,1]\n"
    "v_pk_fma_f32 v[18:19], v[46:47], v[52:53], v[18:19]\n"
    "v_exp_f32 v50, v50\n"
    "v_pk_fma_f32 v[46:47], v[42:43], s[82:83], v[42:43] op_sel:[0,0,1] op_sel_hi:[0,1,1]\n"
    "v_exp_f32 v51, v51\n"
    "v_pk_fma_f32 v[52:53], v[46:47], v[46:47], s[52:53] op_sel_hi:[1,1,0]\n"
    "v_rcp_f32 v54, v54\n"
    "v_pk_mul_f32 v[52:53], v[52:53], v[46:47] neg_lo:[1,0] neg_hi:[1,0]\n"
    "v_rcp_f32 v55, v55\n"
    "v_pk_fma_f32 v[50:51], v[50:51], v[40:41], v[40:41] op_sel:[0,1,1] op_sel_hi:[1,1,1]\n"
    "v_pk_fma_f32 v[20:21], v[48:49], v[54:55], v[20:21]\n"
    "v_exp_f32 v52, v52\n"
    "v_pk_fma_f32 v[48:49], v[42:43], s[84:85], v[42:43] op_sel:[0,0,1] op_sel_hi:[0,1,1]\n"
    "v_exp_f32 v53, v53\n"
    "v_pk_fma_f32 v[54:55], v[48:49], v[48:49], s[52:53] op_sel_hi:[1,1,0]\n"
    "v_rcp_f32 v50, v50\n"
    "v_pk_mul_f32 v[54:55], v[54:55], v[48:49] neg_lo:[1,0] neg_hi:[1,0]\n"
    "v_rcp_f32 v51, v51\n"
    "v_pk_fma_f32 v[52:53], v[52:53], v[40:41], v[40:41] op_sel:[0,1,1] op_sel_hi:[1,1,1]\n"
    "v_pk_fma_f32 v[22:23], v[44:45], v[50:51], v[22:23]\n"
    "v_exp_f32 v54, v54\n"
    "v_pk_fma_f32 v[44:45], v[42:43], s[86:87], v[42:43] op_sel:[0,0,1] op_sel_hi:[0,1,1]\n"
    "v_exp_f32 v55, v55\n"
    "v_pk_fma_f32 v[50:51], v[44:45], v[44:45], s[52:53] op_sel_hi:[1,1,0]\n"
    "v_rcp_f32 v52, v52\n"
    "v_pk_mul_f32 v[50:51], v[50:51], v[44:45] neg_lo:[1,0] neg_hi:[1,0]\n"
    "v_rcp_f32 v53, v53\n"
    "v_pk_fma_f32 v[54:55], v[54:55], v[40:41], v[40:41] op_sel:[0,1,1] op_sel_hi:[1,1,1]\n"
    "v_pk_fma_f32 v[24:25], v[46:47], v[52:53], v[24:25]\n"
    "v_exp_f32 v50, v50\n"
    "v_pk_fma_f32 v[46:47], v[42:43], s[88:89], v[42:43] op_sel:[0,0,1] op_sel_hi:[0,1,1]\n"
    "v_exp_f32 v51, v51\n"
    "v_pk_fma_f32 v[52:53], v[46:47], v[46:47], s[52:53] op_sel_hi:[1,1,0]\n"
    "v_rcp_f32 v54, v54\n"
    "v_pk_mul_f32 v[52:53], v[52:53], v[46:47] neg_lo:[1,0] neg_hi:[1,0]\n"
    "v_rcp_f32 v55, v55\n"
    "v_pk_fma_f32 v[50:51], v[50:51], v[40:41], v[40:41] op_sel:[0,1,1] op_sel_hi:[1,1,1]\n"
    "v_pk_fma_f32 v[26:27], v[48:49], v[54:55], v[26:27]\n"
    "v_exp_f32 v52, v52\n"
    "v_pk_fma_f32 v[48:49], v[42:43], s[90:91], v[42:43] op_sel:[0,0,1] op_sel_hi:[0,1,1]\n"
    "v_exp_f32 v53, v53\n"
    "v_pk_fma_f32 v[54:55], v[48:49], v[48:49], s[52:53] op_sel_hi:[1,1,0]\n"
    "v_rcp_f32 v50, v50\n"
    "v_pk_mul_f32 v[54:55], v[54:55], v[48:49] neg_lo:[1,0] neg_hi:[1,0]\n"
    "v_rcp_f32 v51, v51\n"
    "v_pk_fma_f32 v[52:53], v[52:53], v[40:41], v[40:41] op_sel:[0,1,1] op_sel_hi:[1,1,1]\n"
    "v_pk_fma_f32 v[28:29], v[44:45], v[50:51], v[28:29]\n"
    "v_exp_f32 v54, v54\n"
    "v_pk_fma_f32 v[44:45], v[42:43], s[92:93], v[42:43] op_sel:[0,0,1] op_sel_hi:[0,1,1]\n"
    "v_exp_f32 v55, v55\n"
    "v_pk_fma_f32 v[50:51], v[44:45], v[44:45], s[52:53] op_sel_hi:[1,1,0]\n"
    "v_rcp_f32 v52, v52\n"
    "v_pk_mul_f32 v[50:51], v[50:51], v[44:45] neg_lo:[1,0] neg_hi:[1,0]\n"
    "v_rcp_f32 v53, v53\n"
    "v_pk_fma_f32 v[54:55], v[54:55], v[40:41], v[40:41] op_sel:[0,1,1] op_sel_hi:[1,1,1]\n"
    "v_pk_fma_f32 v[30:31], v[46:47], v[52:53], v[30:31]\n"
    "v_exp_f32 v50, v50\n"
    "v_pk_fma_f32 v[46:47], v[42:43], s[94:95], v[42:43] op_sel:[0,0,1] op_sel_hi:[0,1,1]\n"
    "v_exp_f32 v51, v51\n"
    "v_pk_fma_f32 v[52:53], v[46:47], v[46:47], s[52:53] op_sel_hi:[1,1,0]\n"
    "v_rcp_f32 v54, v54\n"
    "v_pk_mul_f32 v[52:53], v[52:53], v[46:47] neg_lo:[1,0] neg_hi:[1,0]\n"
    "v_rcp_f32 v55, v55\n"
    "v_pk_fma_f32 v[50:51], v[50:51], v[40:41], v[40:41] op_sel:[0,1,1] op_sel_hi:[1,1,1]\n"
    "v_pk_fma_f32 v[32:33], v[48:49], v[54:55], v[32:33]\n"
    "v_exp_f32 v52, v52\n"
    "v_exp_f32 v53, v53\n"
    "v_rcp_f32 v50, v50\n"
    "v_rcp_f32 v51, v51\n"
    "v_pk_fma_f32 v[52:53], v[52:53], v[40:41], v[40:41] op_sel:[0,1,1] op_sel_hi:[1,1,1]\n"
    "v_pk_fma_f32 v[34:35], v[44:45], v[50:51], v[34:35]\n"
    "v_rcp_f32 v52, v52\n"
    "v_rcp_f32 v53, v53\n"
    "s_nop 0\n"
    "v_pk_fma_f32 v[36:37], v[46:47], v[52:53], v[36:37]\n"
    "s_setpc_b64 s[44:45]\n"
    ".Lvrest:\n"
    "s_swappc_b64 s[36:37], s[34:35]\n"
    "s_cmp_gt_u32 s31, 8\n"
    "s_cselect_b32 s64, 9, 0\n"
    "s_lshl_b32 s65, s41, 3\n"
    "s_cmp_gt_u32 s64, s65\n"
    "s_cbranch_scc1 .Lvr1\n"
    "s_sub_u32 s64, s31, s64\n"
    "s_swappc_b64 s[66:67], s[96:97]\n"
    ".Lvr1:\n"
    "s_cmp_lg_u32 s41, 0\n"
    "s_cbranch_scc1 .Lvr2\n"
    "v_lshlrev_b32 v8, 2, v4\n"
    "s_add_u32 s64, s73, 1445888\n"
    "buffer_load_dwordx4 v[44:47], v8, s[16:19], s64 offen offset:0 sc1\n"
    "buffer_load_dwordx4 v[48:51], v8, s[16:19], s64 offen offset:16 sc1\n"
    "buffer_load_dwordx4 v[52:55], v8, s[16:19], s64 offen offset:32 sc1\n"
    ".Lvr2:\n"
    "s_swappc_b64 s[36:37], s[34:35]\n"
    "s_swappc_b64 s[36:37], s[34:35]\n"
    "s_swappc_b64 s[36:37], s[34:35]\n"
    "s_swappc_b64 s[36:37], s[34:35]\n"
    "s_swappc_b64 s[36:37], s[34:35]\n"
    "s_cmp_eq_u32 s41, 0\n"
    "s_cbranch_scc1 .Ltabg\n"
    ".Ltabg_r:\n"
    "s_swappc_b64 s[36:37], s[34:35]\n"
    "s_swappc_b64 s[36:37], s[34:35]\n"
    "s_swappc_b64 s[36:37], s[34:35]\n"
    "s_cmp_eq_u32 s64, s64\n"
    "s_branch .Lpv\n"
    ".Lwof:\n"
    "v_bfe_u32 v8, v4, 7, 1\n"
    "v_bfe_u32 v9, v4, 8, 1\n"
    "v_lshl_or_b32 v8, v8, 3, v9\n"
    "v_lshrrev_b32 v9, 9, v4\n"
    "v_lshl_or_b32 v8, v9, 2, v8\n"
    "v_mul_u32_u24 v8, 1152, v8\n"
    "v_and_b32 v9, 0x70, v4\n"
    "v_add_u32 v8, v8, v9\n"
    "s_lshl_b32 s64, s64, 7\n"
    "s_lshl_b32 s65, s30, 2\n"
    "s_add_u32 s65, s65, s28\n"
    "s_mul_i32 s65, s65, 73728\n"
    "s_add_u32 s64, s64, s65\n"
    "s_add_u32 m0, s42, s73\n"
    "s_sub_u32 m0, m0, 49152\n"
    "s_nop 0\n"
    "buffer_load_dwordx4 v8, s[20:23], s64 offen  lds\n"
    "s_add_u32 m0, m0, 1024\n"
    "s_add_u32 s64, s64, 2304\n"
    "buffer_load_dwordx4 v8, s[20:23], s64 offen  lds\n"
    "s_add_u32 m0, m0, 1024\n"
    "s_add_u32 s64, s64, 16128\n"
    "buffer_load_dwordx4 v8, s[20:23], s64 offen  lds\n"
    "s_add_u32 m0, m0, 1024\n"
    "s_add_u32 s64, s64, 2304\n"
    "buffer_load_dwordx4 v8, s[20:23], s64 offen  lds\n"
    "s_add_u32 m0, m0, 13312\n"
    "s_add_u32 s64, s64, 16128\n"
    "buffer_load_dwordx4 v8, s[20:23], s64 offen  lds\n"
    "s_add_u32 m0, m0, 1024\n"
    "s_add_u32 s64, s64, 2304\n"
    "buffer_load_dwordx4 v8, s[20:23], s64 offen  lds\n"
    "s_add_u32 m0, m0, 1024\n"
    "s_add_u32 s64, s64, 16128\n"
    "buffer_load_dwordx4 v8, s[20:23], s64 offen  lds\n"
    "s_add_u32 m0, m0, 1024\n"
    "s_add_u32 s64, s64, 2304\n"
    "buffer_load_dwordx4 v8, s[20:23], s64 offen  lds\n"
    "s_setpc_b64 s[66:67]\n"
    ".Ltabg:\n"
    "v_max3_u32 v9, v44, v45, v46\n"
    "v_max_u32 v9, v9, v47\n"
    "v_max3_u32 v9, v9, v48, v49\n"
    "v_max3_u32 v9, v9, v50, v51\n"
    "v_max3_u32 v9, v9, v52, v53\n"
    "v_max3_u32 v9, v9, v54, v55\n"
    "v_cmp_eq_u32 vcc, -1, v9\n"
    "s_and_b32 s64, s28, 1\n"
    "s_lshl_b32 s64, s64, 8\n"
    "s_and_b32 s65, s28, 2\n"
    "s_add_u32 s64, s64, s65\n"
    "s_add_u32 s64, s64, 65536\n"
    "v_add_u32 v9, s64, v14\n"
    "s_cbranch_vccnz .Ltg_re\n"
    ".Ltg_ok:\n"
    "ds_write_b16 v9, v44 offset:0\n"
    "ds_write_b16_d16_hi v9, v44 offset:512\n"
    "ds_write_b16 v9, v45 offset:1024\n"
    "ds_write_b16_d16_hi v9, v45 offset:1536\n"
    "ds_write_b16 v9, v46 offset:2048\n"
    "ds_write_b16_d16_hi v9, v46 offset:2560\n"
    "ds_write_b16 v9, v47 offset:3072\n"
    "ds_write_b16_d16_hi v9, v47 offset:3584\n"
    "ds_write_b16 v9, v48 offset:4096\n"
    "ds_write_b16_d16_hi v9, v48 offset:4608\n"
    "ds_write_b16 v9, v49 offset:5120\n"
    "ds_write_b16_d16_hi v9, v49 offset:5632\n"
    "ds_write_b16 v9, v50 offset:6144\n"
    "ds_write_b16_d16_hi v9, v50 offset:6656\n"
    "ds_write_b16 v9, v51 offset:7168\n"
    "ds_write_b16_d16_hi v9, v51 offset:7680\n"
    "ds_write_b16 v9, v52 offset:8192\n"
    "ds_write_b16_d16_hi v9, v52 offset:8704\n"
    "ds_write_b16 v9, v53 offset:9216\n"
    "ds_write_b16_d16_hi v9, v53 offset:9728\n"
    "ds_write_b16 v9, v54 offset:10240\n"
    "ds_write_b16_d16_hi v9, v54 offset:10752\n"
    "s_branch .Ltabg_r\n"
    ".Lwo2:\n"
    "s_sub_u32 s64, s31, 9\n"
    "s_cmp_gt_u32 s64, 1\n"
    "s_cbranch_scc1 .Lnotask\n"
    "s_add_u32 s64, s64, 7\n"
    "s_swappc_b64 s[66:67], s[96:97]\n"
    "s_branch .Lnotask\n"
    ".Ldrv2:\n"
    "s_mov_b32 s41, 2\n"
    "s_swappc_b64 s[36:37], s[34:35]\n"
    "s_barrier\n"
    "s_mov_b32 s64, 15\n"
    ".Ldrv2_l:\n"
    "s_swappc_b64 s[36:37], s[34:35]\n"
    "s_sub_u32 s64, s64, 1\n"
    "s_cbranch_scc0 .Ldrv2_l\n"
    "s_branch .Lloop\n"
    ".Lpre0:\n"
    "s_getpc_b64 s[34:35]\n"
    ".Lpc1815_.Lstream:\n"
    "s_add_u32 s34, s34, .Lstream-.Lpc1815_.Lstream\n"
    "s_addc_u32 s35, s35, 0\n"
    "s_add_u32 s32, s46, 67108864\n"
    "s_add_u32 s33, s73, 98304\n"
    "s_swappc_b64 s[36:37], s[34:35]\n"
    "s_branch .Lnotask\n"
    ".Lpre1:\n"
    "s_getpc_b64 s[34:35]\n"
    ".Lpc1824_.Lstream:\n"
    "s_add_u32 s34, s34, .Lstream-.Lpc1824_.Lstream\n"
    "s_addc_u32 s35, s35, 0\n"
    "s_add_u32 s32, s46, 67108864\n"
    "s_add_u32 s33, s73, 98304\n"
    "s_branch .Lpre1_r\n"
    ".Lpre2:\n"
    "s_swappc_b64 s[36:37], s[34:35]\n"
    "s_swappc_b64 s[36:37], s[34:35]\n"
    "s_branch .Lh2\n"
    ".Lstream:\n"
    "s_add_u32 s72, s32, 204800\n"
    "s_add_u32 m0, s33, 32768\n"
    "s_nop 0\n"
    "buffer_load_dwordx4 v1, s[8:11], s72 offen offset:0 nt lds\n"
    "s_add_u32 m0, m0, 896\n"
    "s_nop 0\n"
    "buffer_load_dwordx4 v1, s[8:11], s72 offen offset:128 nt lds\n"
    "s_add_u32 m0, m0, 640\n"
    "s_nop 0\n"
    "buffer_load_dwordx4 v1, s[8:11], s72 offen offset:512 nt lds\n"
    "s_add_u32 m0, m0, 896\n"
    "s_nop 0\n"
    "buffer_load_dwordx4 v1, s[8:11], s72 offen offset:640 nt lds\n"
    "s_swappc_b64 s[34:35], s[36:37]\n"
    "s_add_u32 s72, s32, 196608\n"
    "s_add_u32 m0, s33, 0\n"
    "s_nop 0\n"
    "buffer_load_dwordx4 v1, s[8:11], s72 offen offset:0 nt lds\n"
    "s_add_u32 m0, m0, 896\n"
    "s_nop 0\n"
    "buffer_load_dwordx4 v1, s[8:11], s72 offen offset:128 nt lds\n"
    "s_add_u32 m0, m0, 640\n"
    "s_nop 0\n"
    "buffer_load_dwordx4 v1, s[8:11], s72 offen offset:512 nt lds\n"
    "s_add_u32 m0, m0, 896\n"
    "s_nop 0\n"
    "buffer_load_dwordx4 v1, s[8:11], s72 offen offset:640 nt lds\n"
    "s_swappc_b64 s[34:35], s[36:37]\n"
    "s_add_u32 s72, s32, 200704\n"
    "s_add_u32 m0, s33, 16384\n"
    "s_nop 0\n"
    "buffer_load_dwordx4 v1, s[8:11], s72 offen offset:0 nt lds\n"
    "s_add_u32 m0, m0, 896\n"
    "s_nop 0\n"
    "buffer_load_dwordx4 v1, s[8:11], s72 offen offset:128 nt lds\n"
    "s_add_u32 m0, m0, 640\n"
    "s_nop 0\n"
    "buffer_load_dwordx4 v1, s[8:11], s72 offen offset:512 nt lds\n"
    "s_add_u32 m0, m0, 896\n"
    "s_nop 0\n"
    "buffer_load_dwordx4 v1, s[8:11], s72 offen offset:640 nt lds\n"
    "s_swappc_b64 s[34:35], s[36:37]\n"
    "s_add_u32 s72, s32, 0\n"
    "buffer_load_dwordx4 v[64:67], v2, s[8:11], s72 offen offset:0 nt\n"
    "buffer_load_dwordx4 v[72:75], v2, s[8:11], s72 offen offset:128 nt\n"
    "buffer_load_dwordx4 v[68:71], v2, s[8:11], s72 offen offset:512 nt\n"
    "buffer_load_dwordx4 v[76:79], v2, s[8:11], s72 offen offset:640 nt\n"
    "s_swappc_b64 s[34:35], s[36:37]\n"
    "s_add_u32 s72, s32, 65536\n"
    "buffer_load_dwordx4 v[80:83], v2, s[8:11], s72 offen offset:0 nt\n"
    "buffer_load_dwordx4 v[88:91], v2, s[8:11], s72 offen offset:128 nt\n"
    "buffer_load_dwordx4 v[84:87], v2, s[8:11], s72 offen offset:512 nt\n"
    "buffer_load_dwordx4 v[92:95], v2, s[8:11], s72 offen offset:640 nt\n"
    "s_swappc_b64 s[34:35], s[36:37]\n"
    "s_add_u32 s72, s32, 131072\n"
    "buffer_load_dwordx4 v[96:99], v2, s[8:11], s72 offen offset:0 nt\n"
    "buffer_load_dwordx4 v[104:107], v2, s[8:11], s72 offen offset:128 nt\n"
    "buffer_load_dwordx4 v[100:103], v2, s[8:11], s72 offen offset:512 nt\n"
    "buffer_load_dwordx4 v[108:111], v2, s[8:11], s72 offen offset:640 nt\n"
    "s_swappc_b64 s[34:35], s[36:37]\n"
    "s_waitcnt vmcnt(16)\n"
    "s_add_u32 s72, s32, 208896\n"
    "s_add_u32 m0, s33, 49152\n"
    "s_nop 0\n"
    "buffer_load_dwordx4 v1, s[8:11], s72 offen offset:0 nt lds\n"
    "s_add_u32 m0, m0, 896\n"
    "s_nop 0\n"
    "buffer_load_dwordx4 v1, s[8:11], s72 offen offset:128 nt lds\n"
    "s_add_u32 m0, m0, 640\n"
    "s_nop 0\n"
    "buffer_load_dwordx4 v1, s[8:11], s72 offen offset:512 nt lds\n"
    "s_add_u32 m0, m0, 896\n"
    "s_nop 0\n"
    "buffer_load_dwordx4 v1, s[8:11], s72 offen offset:640 nt lds\n"
    "s_swappc_b64 s[34:35], s[36:37]\n"
    "s_waitcnt vmcnt(16)\n"
    "s_add_u32 s72, s32, 4096\n"
    "buffer_load_dwordx4 v[112:115], v2, s[8:11], s72 offen offset:0 nt\n"
    "buffer_load_dwordx4 v[120:123], v2, s[8:11], s72 offen offset:128 nt\n"
    "buffer_load_dwordx4 v[116:119], v2, s[8:11], s72 offen offset:512 nt\n"
    "buffer_load_dwordx4 v[124:127], v2, s[8:11], s72 offen offset:640 nt\n"
    "s_swappc_b64 s[34:35], s[36:37]\n"
    "s_waitcnt vmcnt(16)\n"
    "s_add_u32 s72, s32, 69632\n"
    "buffer_load_dwordx4 v[128:131], v2, s[8:11], s72 offen offset:0 nt\n"
    "buffer_load_dwordx4 v[136:139], v2, s[8:11], s72 offen offset:128 nt\n"
    "buffer_load_dwordx4 v[132:135], v2, s[8:11], s72 offen offset:512 nt\n"
    "buffer_load_dwordx4 v[140:143], v2, s[8:11], s72 offen offset:640 nt\n"
    "v_mov_b64 v[60:61], v[64:65]\n"
    "v_mov_b64 v[62:63], v[66:67]\n"
    "v_mov_b32_dpp v64, v68 row_ror:8 row_mask:0xf bank_mask:0xc\n"
    "v_mov_b32_dpp v65, v69 row_ror:8 row_mask:0xf bank_mask:0xc\n"
    "v_mov_b32_dpp v66, v70 row_ror:8 row_mask:0xf bank_mask:0xc\n"
    "v_mov_b32_dpp v67, v71 row_ror:8 row_mask:0xf bank_mask:0xc\n"
    "v_mov_b32_dpp v68, v60 row_ror:8 row_mask:0xf bank_mask:0x3\n"
    "v_mov_b32_dpp v69, v61 row_ror:8 row_mask:0xf bank_mask:0x3\n"
    "v_mov_b32_dpp v70, v62 row_ror:8 row_mask:0xf bank_mask:0x3\n"
    "v_mov_b32_dpp v71, v63 row_ror:8 row_mask:0xf bank_mask:0x3\n"
    "v_mov_b64 v[60:61], v[72:73]\n"
    "v_mov_b64 v[62:63], v[74:75]\n"
    "v_mov_b32_dpp v72, v76 row_ror:8 row_mask:0xf bank_mask:0xc\n"
    "v_mov_b32_dpp v73, v77 row_ror:8 row_mask:0xf bank_mask:0xc\n"
    "v_mov_b32_dpp v74, v78 row_ror:8 row_mask:0xf bank_mask:0xc\n"
    "v_mov_b32_dpp v75, v79 row_ror:8 row_mask:0xf bank_mask:0xc\n"
    "v_mov_b32_dpp v76, v60 row_ror:8 row_mask:0xf bank_mask:0x3\n"
    "v_mov_b32_dpp v77, v61 row_ror:8 row_mask:0xf bank_mask:0x3\n"
    "v_mov_b32_dpp v78, v62 row_ror:8 row_mask:0xf bank_mask:0x3\n"
    "v_mov_b32_dpp v79, v63 row_ror:8 row_mask:0xf bank_mask:0x3\n"
    "s_nop 0\n"
    "s_swappc_b64 s[34:35], s[36:37]\n"
    "s_waitcnt vmcnt(16)\n"
    "s_add_u32 s72, s32, 135168\n"
    "buffer_load_dwordx4 v[144:147], v2, s[8:11], s72 offen offset:0 nt\n"
    "buffer_load_dwordx4 v[152:155], v2, s[8:11], s72 offen offset:128 nt\n"
    "buffer_load_dwordx4 v[148:151], v2, s[8:11], s72 offen offset:512 nt\n"
    "buffer_load_dwordx4 v[156:159], v2, s[8:11], s72 offen offset:640 nt\n"
    "v_mov_b64 v[60:61], v[80:81]\n"
    "v_mov_b64 v[62:63], v[82:83]\n"
    "v_mov_b32_dpp v80, v84 row_ror:8 row_mask:0xf bank_mask:0xc\n"
    "v_mov_b32_dpp v81, v85 row_ror:8 row_mask:0xf bank_mask:0xc\n"
    "v_mov_b32_dpp v82, v86 row_ror:8 row_mask:0xf bank_mask:0xc\n"
    "v_mov_b32_dpp v83, v87 row_ror:8 row_mask:0xf bank_mask:0xc\n"
    "v_mov_b32_dpp v84, v60 row_ror:8 row_mask:0xf bank_mask:0x3\n"
    "v_mov_b32_dpp v85, v61 row_ror:8 row_mask:0xf bank_mask:0x3\n"
    "v_mov_b32_dpp v86, v62 row_ror:8 row_mask:0xf bank_mask:0x3\n"
    "v_mov_b32_dpp v87, v63 row_ror:8 row_mask:0xf bank_mask:0x3\n"
    "v_mov_b64 v[60:61], v[88:89]\n"
    "v_mov_b64 v[62:63], v[90:91]\n"
    "v_mov_b32_dpp v88, v92 row_ror:8 row_mask:0xf bank_mask:0xc\n"
    "v_mov_b32_dpp v89, v93 row_ror:8 row_mask:0xf bank_mask:0xc\n"
    "v_mov_b32_dpp v90, v94 row_ror:8 row_mask:0xf bank_mask:0xc\n"
    "v_mov_b32_dpp v91, v95 row_ror:8 row_mask:0xf bank_mask:0xc\n"
    "v_mov_b32_dpp v92, v60 row_ror:8 row_mask:0xf bank_mask:0x3\n"
    "v_mov_b32_dpp v93, v61 row_ror:8 row_mask:0xf bank_mask:0x3\n"
    "v_mov_b32_dpp v94, v62 row_ror:8 row_mask:0xf bank_mask:0x3\n"
    "v_mov_b32_dpp v95, v63 row_ror:8 row_mask:0xf bank_mask:0x3\n"
    "s_nop 0\n"
    "s_swappc_b64 s[34:35], s[36:37]\n"
    "s_waitcnt vmcnt(16)\n"
    "s_add_u32 s72, s32, 8192\n"
    "buffer_load_dwordx4 v[160:163], v2, s[8:11], s72 offen offset:0 nt\n"
    "buffer_load_dwordx4 v[168:171], v2, s[8:11], s72 offen offset:128 nt\n"
    "buffer_load_dwordx4 v[164:167], v2, s[8:11], s72 offen offset:512 nt\n"
    "buffer_load_dwordx4 v[172:175], v2, s[8:11], s72 offen offset:640 nt\n"
    "v_mov_b64 v[60:61], v[96:97]\n"
    "v_mov_b64 v[62:63], v[98:99]\n"
    "v_mov_b32_dpp v96, v100 row_ror:8 row_mask:0xf bank_mask:0xc\n"
    "v_mov_b32_dpp v97, v101 row_ror:8 row_mask:0xf bank_mask:0xc\n"
    "v_mov_b32_dpp v98, v102 row_ror:8 row_mask:0xf bank_mask:0xc\n"
    "v_mov_b32_dpp v99, v103 row_ror:8 row_mask:0xf bank_mask:0xc\n"
    "v_mov_b32_dpp v100, v60 row_ror:8 row_mask:0xf bank_mask:0x3\n"
    "v_mov_b32_dpp v101, v61 row_ror:8 row_mask:0xf bank_mask:0x3\n"
    "v_mov_b32_dpp v102, v62 row_ror:8 row_mask:0xf bank_mask:0x3\n"
    "v_mov_b32_dpp v103, v63 row_ror:8 row_mask:0xf bank_mask:0x3\n"
    "v_mov_b64 v[60:61], v[104:105]\n"
    "v_mov_b64 v[62:63], v[106:107]\n"
    "v_mov_b32_dpp v104, v108 row_ror:8 row_mask:0xf bank_mask:0xc\n"
    "v_mov_b32_dpp v105, v109 row_ror:8 row_mask:0xf bank_mask:0xc\n"
    "v_mov_b32_dpp v106, v110 row_ror:8 row_mask:0xf bank_mask:0xc\n"
    "v_mov_b32_dpp v107, v111 row_ror:8 row_mask:0xf bank_mask:0xc\n"
    "v_mov_b32_dpp v108, v60 row_ror:8 row_mask:0xf bank_mask:0x3\n"
    "v_mov_b32_dpp v109, v61 row_ror:8 row_mask:0xf bank_mask:0x3\n"
    "v_mov_b32_dpp v110, v62 row_ror:8 row_mask:0xf bank_mask:0x3\n"
    "v_mov_b32_dpp v111, v63 row_ror:8 row_mask:0xf bank_mask:0x3\n"
    "s_nop 0\n"
    "s_swappc_b64 s[34:35], s[36:37]\n"
    "s_waitcnt vmcnt(16)\n"
    "s_add_u32 s72, s32, 73728\n"
    "buffer_load_dwordx4 v[176:179], v2, s[8:11], s72 offen offset:0 nt\n"
    "buffer_load_dwordx4 v[184:187], v2, s[8:11], s72 offen offset:128 nt\n"
    "buffer_load_dwordx4 v[180:183], v2, s[8:11], s72 offen offset:512 nt\n"
    "buffer_load_dwordx4 v[188:191], v2, s[8:11], s72 offen offset:640 nt\n"
    "s_swappc_b64 s[34:35], s[36:37]\n"
    "s_waitcnt vmcnt(16)\n"
    "s_add_u32 s72, s32, 139264\n"
    "buffer_load_dwordx4 v[192:195], v2, s[8:11], s72 offen offset:0 nt\n"
    "buffer_load_dwordx4 v[200:203], v2, s[8:11], s72 offen offset:128 nt\n"
    "buffer_load_dwordx4 v[196:199], v2, s[8:11], s72 offen offset:512 nt\n"
    "buffer_load_dwordx4 v[204:207], v2, s[8:11], s72 offen offset:640 nt\n"
    "v_mov_b64 v[60:61], v[112:113]\n"
    "v_mov_b64 v[62:63], v[114:115]\n"
    "v_mov_b32_dpp v112, v116 row_ror:8 row_mask:0xf bank_mask:0xc\n"
    "v_mov_b32_dpp v113, v117 row_ror:8 row_mask:0xf bank_mask:0xc\n"
    "v_mov_b32_dpp v114, v118 row_ror:8 row_mask:0xf bank_mask:0xc\n"
    "v_mov_b32_dpp v115, v119 row_ror:8 row_mask:0xf bank_mask:0xc\n"
    "v_mov_b32_dpp v116, v60 row_ror:8 row_mask:0xf bank_mask:0x3\n"
    "v_mov_b32_dpp v117, v61 row_ror:8 row_mask:0xf bank_mask:0x3\n"
    "v_mov_b32_dpp v118, v62 row_ror:8 row_mask:0xf bank_mask:0x3\n"
    "v_mov_b32_dpp v119, v63 row_ror:8 row_mask:0xf bank_mask:0x3\n"
    "v_mov_b64 v[60:61], v[120:121]\n"
    "v_mov_b64 v[62:63], v[122:123]\n"
    "v_mov_b32_dpp v120, v124 row_ror:8 row_mask:0xf bank_mask:0xc\n"
    "v_mov_b32_dpp v121, v125 row_ror:8 row_mask:0xf bank_mask:0xc\n"
    "v_mov_b32_dpp v122, v126 row_ror:8 row_mask:0xf bank_mask:0xc\n"
    "v_mov_b32_dpp v123, v127 row_ror:8 row_mask:0xf bank_mask:0xc\n"
    "v_mov_b32_dpp v124, v60 row_ror:8 row_mask:0xf bank_mask:0x3\n"
    "v_mov_b32_dpp v125, v61 row_ror:8 row_mask:0xf bank_mask:0x3\n"
    "v_mov_b32_dpp v126, v62 row_ror:8 row_mask:0xf bank_mask:0x3\n"
    "v_mov_b32_dpp v127, v63 row_ror:8 row_mask:0xf bank_mask:0x3\n"
    "s_nop 0\n"
    "s_swappc_b64 s[34:35], s[36:37]\n"
    "s_waitcnt vmcnt(16)\n"
    "s_add_u32 s72, s32, 12288\n"
    "buffer_load_dwordx4 v[208:211], v2, s[8:11], s72 offen offset:0 nt\n"
    "buffer_load_dwordx4 v[216:219], v2, s[8:11], s72 offen offset:128 nt\n"
    "buffer_load_dwordx4 v[212:215], v2, s[8:11], s72 offen offset:512 nt\n"
    "buffer_load_dwordx4 v[220:223], v2, s[8:11], s72 offen offset:640 nt\n"
    "v_mov_b64 v[60:61], v[128:129]\n"
    "v_mov_b64 v[62:63], v[130:131]\n"
    "v_mov_b32_dpp v128, v132 row_ror:8 row_mask:0xf bank_mask:0xc\n"
    "v_mov_b32_dpp v129, v133 row_ror:8 row_mask:0xf bank_mask:0xc\n"
    "v_mov_b32_dpp v130, v134 row_ror:8 row_mask:0xf bank_mask:0xc\n"
    "v_mov_b32_dpp v131, v135 row_ror:8 row_mask:0xf bank_mask:0xc\n"
    "v_mov_b32_dpp v132, v60 row_ror:8 row_mask:0xf bank_mask:0x3\n"
    "v_mov_b32_dpp v133, v61 row_ror:8 row_mask:0xf bank_mask:0x3\n"
    "v_mov_b32_dpp v134, v62 row_ror:8 row_mask:0xf bank_mask:0x3\n"
    "v_mov_b32_dpp v135, v63 row_ror:8 row_mask:0xf bank_mask:0x3\n"
    "v_mov_b64 v[60:61], v[136:137]\n"
    "v_mov_b64 v[62:63], v[138:139]\n"
    "v_mov_b32_dpp v136, v140 row_ror:8 row_mask:0xf bank_mask:0xc\n"
    "v_mov_b32_dpp v137, v141 row_ror:8 row_mask:0xf bank_mask:0xc\n"
    "v_mov_b32_dpp v138, v142 row_ror:8 row_mask:0xf bank_mask:0xc\n"
    "v_mov_b32_dpp v139, v143 row_ror:8 row_mask:0xf bank_mask:0xc\n"
    "v_mov_b32_dpp v140, v60 row_ror:8 row_mask:0xf bank_mask:0x3\n"
    "v_mov_b32_dpp v141, v61 row_ror:8 row_mask:0xf bank_mask:0x3\n"
    "v_mov_b32_dpp v142, v62 row_ror:8 row_mask:0xf bank_mask:0x3\n"
    "v_mov_b32_dpp v143, v63 row_ror:8 row_mask:0xf bank_mask:0x3\n"
    "s_nop 0\n"
    "s_swappc_b64 s[34:35], s[36:37]\n"
    "s_waitcnt vmcnt(16)\n"
    "s_add_u32 s72, s32, 77824\n"
    "buffer_load_dwordx4 v[224:227], v2, s[8:11], s72 offen offset:0 nt\n"
    "buffer_load_dwordx4 v[232:235], v2, s[8:11], s72 offen offset:128 nt\n"
    "buffer_load_dwordx4 v[228:231], v2, s[8:11], s72 offen offset:512 nt\n"
    "buffer_load_dwordx4 v[236:239], v2, s[8:11], s72 offen offset:640 nt\n"
    "v_mov_b64 v[60:61], v[144:145]\n"
    "v_mov_b64 v[62:63], v[146:147]\n"
    "v_mov_b32_dpp v144, v148 row_ror:8 row_mask:0xf bank_mask:0xc\n"
    "v_mov_b32_dpp v145, v149 row_ror:8 row_mask:0xf bank_mask:0xc\n"
    "v_mov_b32_dpp v146, v150 row_ror:8 row_mask:0xf bank_mask:0xc\n"
    "v_mov_b32_dpp v147, v151 row_ror:8 row_mask:0xf bank_mask:0xc\n"
    "v_mov_b32_dpp v148, v60 row_ror:8 row_mask:0xf bank_mask:0x3\n"
    "v_mov_b32_dpp v149, v61 row_ror:8 row_mask:0xf bank_mask:0x3\n"
    "v_mov_b32_dpp v150, v62 row_ror:8 row_mask:0xf bank_mask:0x3\n"
    "v_mov_b32_dpp v151, v63 row_ror:8 row_mask:0xf bank_mask:0x3\n"
    "v_mov_b64 v[60:61], v[152:153]\n"
    "v_mov_b64 v[62:63], v[154:155]\n"
    "v_mov_b32_dpp v152, v156 row_ror:8 row_mask:0xf bank_mask:0xc\n"
    "v_mov_b32_dpp v153, v157 row_ror:8 row_mask:0xf bank_mask:0xc\n"
    "v_mov_b32_dpp v154, v158 row_ror:8 row_mask:0xf bank_mask:0xc\n"
    "v_mov_b32_dpp v155, v159 row_ror:8 row_mask:0xf bank_mask:0xc\n"
    "v_mov_b32_dpp v156, v60 row_ror:8 row_mask:0xf bank_mask:0x3\n"
    "v_mov_b32_dpp v157, v61 row_ror:8 row_mask:0xf bank_mask:0x3\n"
    "v_mov_b32_dpp v158, v62 row_ror:8 row_mask:0xf bank_mask:0x3\n"
    "v_mov_b32_dpp v159, v63 row_ror:8 row_mask:0xf bank_mask:0x3\n"
    "s_nop 0\n"
    "s_swappc_b64 s[34:35], s[36:37]\n"
    "s_waitcnt vmcnt(16)\n"
    "s_add_u32 s72, s32, 143360\n"
    "buffer_load_dwordx4 v[240:243], v2, s[8:11], s72 offen offset:0 nt\n"
    "buffer_load_dwordx4 v[248:251], v2, s[8:11], s72 offen offset:128 nt\n"
    "buffer_load_dwordx4 v[244:247], v2, s[8:11], s72 offen offset:512 nt\n"
    "buffer_load_dwordx4 v[252:255], v2, s[8:11], s72 offen offset:640 nt\n"
    "v_mov_b64 v[60:61], v[160:161]\n"
    "v_mov_b64 v[62:63], v[162:163]\n"
    "v_mov_b32_dpp v160, v164 row_ror:8 row_mask:0xf bank_mask:0xc\n"
    "v_mov_b32_dpp v161, v165 row_ror:8 row_mask:0xf bank_mask:0xc\n"
    "v_mov_b32_dpp v162, v166 row_ror:8 row_mask:0xf bank_mask:0xc\n"
    "v_mov_b32_dpp v163, v167 row_ror:8 row_mask:0xf bank_mask:0xc\n"
    "v_mov_b32_dpp v164, v60 row_ror:8 row_mask:0xf bank_mask:0x3\n"
    "v_mov_b32_dpp v165, v61 row_ror:8 row_mask:0xf bank_mask:0x3\n"
    "v_mov_b32_dpp v166, v62 row_ror:8 row_mask:0xf bank_mask:0x3\n"
    "v_mov_b32_dpp v167, v63 row_ror:8 row_mask:0xf bank_mask:0x3\n"
    "v_mov_b64 v[60:61], v[168:169]\n"
    "v_mov_b64 v[62:63], v[170:171]\n"
    "v_mov_b32_dpp v168, v172 row_ror:8 row_mask:0xf bank_mask:0xc\n"
    "v_mov_b32_dpp v169, v173 row_ror:8 row_mask:0xf bank_mask:0xc\n"
    "v_mov_b32_dpp v170, v174 row_ror:8 row_mask:0xf bank_mask:0xc\n"
    "v_mov_b32_dpp v171, v175 row_ror:8 row_mask:0xf bank_mask:0xc\n"
    "v_mov_b32_dpp v172, v60 row_ror:8 row_mask:0xf bank_mask:0x3\n"
    "v_mov_b32_dpp v173, v61 row_ror:8 row_mask:0xf bank_mask:0x3\n"
    "v_mov_b32_dpp v174, v62 row_ror:8 row_mask:0xf bank_mask:0x3\n"
    "v_mov_b32_dpp v175, v63 row_ror:8 row_mask:0xf bank_mask:0x3\n"
    "s_nop 0\n"
    "s_swappc_b64 s[34:35], s[36:37]\n"
    "s_waitcnt vmcnt(16)\n"
    "s_add_u32 s72, s32, 0\n"
    "buffer_load_dwordx2 a[0:1], v3, s[12:15], s72 offen offset:0 nt\n"
    "buffer_load_dwordx2 a[8:9], v3, s[12:15], s72 offen offset:128 nt\n"
    "buffer_load_dwordx2 a[2:3], v3, s[12:15], s72 offen offset:256 nt\n"
    "buffer_load_dwordx2 a[10:11], v3, s[12:15], s72 offen offset:384 nt\n"
    "s_add_u32 s72, s72, 8192\n"
    "buffer_load_dwordx2 a[4:5], v3, s[12:15], s72 offen offset:0 nt\n"
    "buffer_load_dwordx2 a[12:13], v3, s[12:15], s72 offen offset:128 nt\n"
    "buffer_load_dwordx2 a[6:7], v3, s[12:15], s72 offen offset:256 nt\n"
    "buffer_load_dwordx2 a[14:15], v3, s[12:15], s72 offen offset:384 nt\n"
    "v_mov_b64 v[60:61], v[176:177]\n"
    "v_mov_b64 v[62:63], v[178:179]\n"
    "v_mov_b32_dpp v176, v180 row_ror:8 row_mask:0xf bank_mask:0xc\n"
    "v_mov_b32_dpp v177, v181 row_ror:8 row_mask:0xf bank_mask:0xc\n"
    "v_mov_b32_dpp v178, v182 row_ror:8 row_mask:0xf bank_mask:0xc\n"
    "v_mov_b32_dpp v179, v183 row_ror:8 row_mask:0xf bank_mask:0xc\n"
    "v_mov_b32_dpp v180, v60 row_ror:8 row_mask:0xf bank_mask:0x3\n"
    "v_mov_b32_dpp v181, v61 row_ror:8 row_mask:0xf bank_mask:0x3\n"
    "v_mov_b32_dpp v182, v62 row_ror:8 row_mask:0xf bank_mask:0x3\n"
    "v_mov_b32_dpp v183, v63 row_ror:8 row_mask:0xf bank_mask:0x3\n"
    "v_mov_b64 v[60:61], v[184:185]\n"
    "v_mov_b64 v[62:63], v[186:187]\n"
    "v_mov_b32_dpp v184, v188 row_ror:8 row_mask:0xf bank_mask:0xc\n"
    "v_mov_b32_dpp v185, v189 row_ror:8 row_mask:0xf bank_mask:0xc\n"
    "v_mov_b32_dpp v186, v190 row_ror:8 row_mask:0xf bank_mask:0xc\n"
    "v_mov_b32_dpp v187, v191 row_ror:8 row_mask:0xf bank_mask:0xc\n"
    "v_mov_b32_dpp v188, v60 row_ror:8 row_mask:0xf bank_mask:0x3\n"
    "v_mov_b32_dpp v189, v61 row_ror:8 row_mask:0xf bank_mask:0x3\n"
    "v_mov_b32_dpp v190, v62 row_ror:8 row_mask:0xf bank_mask:0x3\n"
    "v_mov_b32_dpp v191, v63 row_ror:8 row_mask:0xf bank_mask:0x3\n"
    "s_nop 0\n"
    "s_swappc_b64 s[34:35], s[36:37]\n"
    "s_waitcnt vmcnt(20)\n"
    "s_add_u32 s72, s32, 1024\n"
    "buffer_load_dwordx2 a[16:17], v3, s[12:15], s72 offen offset:0 nt\n"
    "buffer_load_dwordx2 a[24:25], v3, s[12:15], s72 offen offset:128 nt\n"
    "buffer_load_dwordx2 a[18:19], v3, s[12:15], s72 offen offset:256 nt\n"
    "buffer_load_dwordx2 a[26:27], v3, s[12:15], s72 offen offset:384 nt\n"
    "s_add_u32 s72, s72, 8192\n"
    "buffer_load_dwordx2 a[20:21], v3, s[12:15], s72 offen offset:0 nt\n"
    "buffer_load_dwordx2 a[28:29], v3, s[12:15], s72 offen offset:128 nt\n"
    "buffer_load_dwordx2 a[22:23], v3, s[12:15], s72 offen offset:256 nt\n"
    "buffer_load_dwordx2 a[30:31], v3, s[12:15], s72 offen offset:384 nt\n"
    "v_mov_b64 v[60:61], v[192:193]\n"
    "v_mov_b64 v[62:63], v[194:195]\n"
    "v_mov_b32_dpp v192, v196 row_ror:8 row_mask:0xf bank_mask:0xc\n"
    "v_mov_b32_dpp v193, v197 row_ror:8 row_mask:0xf bank_mask:0xc\n"
    "v_mov_b32_dpp v194, v198 row_ror:8 row_mask:0xf bank_mask:0xc\n"
    "v_mov_b32_dpp v195, v199 row_ror:8 row_mask:0xf bank_mask:0xc\n"
    "v_mov_b32_dpp v196, v60 row_ror:8 row_mask:0xf bank_mask:0x3\n"
    "v_mov_b32_dpp v197, v61 row_ror:8 row_mask:0xf bank_mask:0x3\n"
    "v_mov_b32_dpp v198, v62 row_ror:8 row_mask:0xf bank_mask:0x3\n"
    "v_mov_b32_dpp v199, v63 row_ror:8 row_mask:0xf bank_mask:0x3\n"
    "v_mov_b64 v[60:61], v[200:201]\n"
    "v_mov_b64 v[62:63], v[202:203]\n"
    "v_mov_b32_dpp v200, v204 row_ror:8 row_mask:0xf bank_mask:0xc\n"
    "v_mov_b32_dpp v201, v205 row_ror:8 row_mask:0xf bank_mask:0xc\n"
    "v_mov_b32_dpp v202, v206 row_ror:8 row_mask:0xf bank_mask:0xc\n"
    "v_mov_b32_dpp v203, v207 row_ror:8 row_mask:0xf bank_mask:0xc\n"
    "v_mov_b32_dpp v204, v60 row_ror:8 row_mask:0xf bank_mask:0x3\n"
    "v_mov_b32_dpp v205, v61 row_ror:8 row_mask:0xf bank_mask:0x3\n"
    "v_mov_b32_dpp v206, v62 row_ror:8 row_mask:0xf bank_mask:0x3\n"
    "v_mov_b32_dpp v207, v63 row_ror:8 row_mask:0xf bank_mask:0x3\n"
    "s_nop 0\n"
    "s_swappc_b64 s[34:35], s[36:37]\n"
    "s_waitcnt vmcnt(24)\n"
    "s_add_u32 s72, s32, 2048\n"
    "buffer_load_dwordx2 a[32:33], v3, s[12:15], s72 offen offset:0 nt\n"
    "buffer_load_dwordx2 a[40:41], v3, s[12:15], s72 offen offset:128 nt\n"
    "buffer_load_dwordx2 a[34:35], v3, s[12:15], s72 offen offset:256 nt\n"
    "buffer_load_dwordx2 a[42:43], v3, s[12:15], s72 offen offset:384 nt\n"
    "s_add_u32 s72, s72, 8192\n"
    "buffer_load_dwordx2 a[36:37], v3, s[12:15], s72 offen offset:0 nt\n"
    "buffer_load_dwordx2 a[44:45], v3, s[12:15], s72 offen offset:128 nt\n"
    "buffer_load_dwordx2 a[38:39], v3, s[12:15], s72 offen offset:256 nt\n"
    "buffer_load_dwordx2 a[46:47], v3, s[12:15], s72 offen offset:384 nt\n"
    "v_mov_b64 v[60:61], v[208:209]\n"
    "v_mov_b64 v[62:63], v[210:211]\n"
    "v_mov_b32_dpp v208, v212 row_ror:8 row_mask:0xf bank_mask:0xc\n"
    "v_mov_b32_dpp v209, v213 row_ror:8 row_mask:0xf bank_mask:0xc\n"
    "v_mov_b32_dpp v210, v214 row_ror:8 row_mask:0xf bank_mask:0xc\n"
    "v_mov_b32_dpp v211, v215 row_ror:8 row_mask:0xf bank_mask:0xc\n"
    "v_mov_b32_dpp v212, v60 row_ror:8 row_mask:0xf bank_mask:0x3\n"
    "v_mov_b32_dpp v213, v61 row_ror:8 row_mask:0xf bank_mask:0x3\n"
    "v_mov_b32_dpp v214, v62 row_ror:8 row_mask:0xf bank_mask:0x3\n"
    "v_mov_b32_dpp v215, v63 row_ror:8 row_mask:0xf bank_mask:0x3\n"
    "v_mov_b64 v[60:61], v[216:217]\n"
    "v_mov_b64 v[62:63], v[218:219]\n"
    "v_mov_b32_dpp v216, v220 row_ror:8 row_mask:0xf bank_mask:0xc\n"
    "v_mov_b32_dpp v217, v221 row_ror:8 row_mask:0xf bank_mask:0xc\n"
    "v_mov_b32_dpp v218, v222 row_ror:8 row_mask:0xf bank_mask:0xc\n"
    "v_mov_b32_dpp v219, v223 row_ror:8 row_mask:0xf bank_mask:0xc\n"
    "v_mov_b32_dpp v220, v60 row_ror:8 row_mask:0xf bank_mask:0x3\n"
    "v_mov_b32_dpp v221, v61 row_ror:8 row_mask:0xf bank_mask:0x3\n"
    "v_mov_b32_dpp v222, v62 row_ror:8 row_mask:0xf bank_mask:0x3\n"
    "v_mov_b32_dpp v223, v63 row_ror:8 row_mask:0xf bank_mask:0x3\n"
    "s_nop 0\n"
    "s_swappc_b64 s[34:35], s[36:37]\n"
    "s_waitcnt vmcnt(28)\n"
    "s_add_u32 s72, s32, 3072\n"
    "buffer_load_dwordx2 a[48:49], v3, s[12:15], s72 offen offset:0 nt\n"
    "buffer_load_dwordx2 a[56:57], v3, s[12:15], s72 offen offset:128 nt\n"
    "buffer_load_dwordx2 a[50:51], v3, s[12:15], s72 offen offset:256 nt\n"
    "buffer_load_dwordx2 a[58:59], v3, s[12:15], s72 offen offset:384 nt\n"
    "s_add_u32 s72, s72, 8192\n"
    "buffer_load_dwordx2 a[52:53], v3, s[12:15], s72 offen offset:0 nt\n"
    "buffer_load_dwordx2 a[60:61], v3, s[12:15], s72 offen offset:128 nt\n"
    "buffer_load_dwordx2 a[54:55], v3, s[12:15], s72 offen offset:256 nt\n"
    "buffer_load_dwordx2 a[62:63], v3, s[12:15], s72 offen offset:384 nt\n"
    "v_mov_b64 v[60:61], v[224:225]\n"
    "v_mov_b64 v[62:63], v[226:227]\n"
    "v_mov_b32_dpp v224, v228 row_ror:8 row_mask:0xf bank_mask:0xc\n"
    "v_mov_b32_dpp v225, v229 row_ror:8 row_mask:0xf bank_mask:0xc\n"
    "v_mov_b32_dpp v226, v230 row_ror:8 row_mask:0xf bank_mask:0xc\n"
    "v_mov_b32_dpp v227, v231 row_ror:8 row_mask:0xf bank_mask:0xc\n"
    "v_mov_b32_dpp v228, v60 row_ror:8 row_mask:0xf bank_mask:0x3\n"
    "v_mov_b32_dpp v229, v61 row_ror:8 row_mask:0xf bank_mask:0x3\n"
    "v_mov_b32_dpp v230, v62 row_ror:8 row_mask:0xf bank_mask:0x3\n"
    "v_mov_b32_dpp v231, v63 row_ror:8 row_mask:0xf bank_mask:0x3\n"
    "v_mov_b64 v[60:61], v[232:233]\n"
    "v_mov_b64 v[62:63], v[234:235]\n"
    "v_mov_b32_dpp v232, v236 row_ror:8 row_mask:0xf bank_mask:0xc\n"
    "v_mov_b32_dpp v233, v237 row_ror:8 row_mask:0xf bank_mask:0xc\n"
    "v_mov_b32_dpp v234, v238 row_ror:8 row_mask:0xf bank_mask:0xc\n"
    "v_mov_b32_dpp v235, v239 row_ror:8 row_mask:0xf bank_mask:0xc\n"
    "v_mov_b32_dpp v236, v60 row_ror:8 row_mask:0xf bank_mask:0x3\n"
    "v_mov_b32_dpp v237, v61 row_ror:8 row_mask:0xf bank_mask:0x3\n"
    "v_mov_b32_dpp v238, v62 row_ror:8 row_mask:0xf bank_mask:0x3\n"
    "v_mov_b32_dpp v239, v63 row_ror:8 row_mask:0xf bank_mask:0x3\n"
    "s_nop 0\n"
    "s_swappc_b64 s[34:35], s[36:37]\n"
    "s_waitcnt vmcnt(32)\n"
    "s_add_u32 s72, s32, 65536\n"
    "buffer_load_dwordx2 a[64:65], v3, s[12:15], s72 offen offset:0 nt\n"
    "buffer_load_dwordx2 a[72:73], v3, s[12:15], s72 offen offset:128 nt\n"
    "buffer_load_dwordx2 a[66:67], v3, s[12:15], s72 offen offset:256 nt\n"
    "buffer_load_dwordx2 a[74:75], v3, s[12:15], s72 offen offset:384 nt\n"
    "s_add_u32 s72, s72, 8192\n"
    "buffer_load_dwordx2 a[68:69], v3, s[12:15], s72 offen offset:0 nt\n"
    "buffer_load_dwordx2 a[76:77], v3, s[12:15], s72 offen offset:128 nt\n"
    "buffer_load_dwordx2 a[70:71], v3, s[12:15], s72 offen offset:256 nt\n"
    "buffer_load_dwordx2 a[78:79], v3, s[12:15], s72 offen offset:384 nt\n"
    "v_mov_b64 v[60:61], v[240:241]\n"
    "v_mov_b64 v[62:63], v[242:243]\n"
    "v_mov_b32_dpp v240, v244 row_ror:8 row_mask:0xf bank_mask:0xc\n"
    "v_mov_b32_dpp v241, v245 row_ror:8 row_mask:0xf bank_mask:0xc\n"
    "v_mov_b32_dpp v242, v246 row_ror:8 row_mask:0xf bank_mask:0xc\n"
    "v_mov_b32_dpp v243, v247 row_ror:8 row_mask:0xf bank_mask:0xc\n"
    "v_mov_b32_dpp v244, v60 row_ror:8 row_mask:0xf bank_mask:0x3\n"
    "v_mov_b32_dpp v245, v61 row_ror:8 row_mask:0xf bank_mask:0x3\n"
    "v_mov_b32_dpp v246, v62 row_ror:8 row_mask:0xf bank_mask:0x3\n"
    "v_mov_b32_dpp v247, v63 row_ror:8 row_mask:0xf bank_mask:0x3\n"
    "v_mov_b64 v[60:61], v[248:249]\n"
    "v_mov_b64 v[62:63], v[250:251]\n"
    "v_mov_b32_dpp v248, v252 row_ror:8 row_mask:0xf bank_mask:0xc\n"
    "v_mov_b32_dpp v249, v253 row_ror:8 row_mask:0xf bank_mask:0xc\n"
    "v_mov_b32_dpp v250, v254 row_ror:8 row_mask:0xf bank_mask:0xc\n"
    "v_mov_b32_dpp v251, v255 row_ror:8 row_mask:0xf bank_mask:0xc\n"
    "v_mov_b32_dpp v252, v60 row_ror:8 row_mask:0xf bank_mask:0x3\n"
    "v_mov_b32_dpp v253, v61 row_ror:8 row_mask:0xf bank_mask:0x3\n"
    "v_mov_b32_dpp v254, v62 row_ror:8 row_mask:0xf bank_mask:0x3\n"
    "v_mov_b32_dpp v255, v63 row_ror:8 row_mask:0xf bank_mask:0x3\n"
    "s_nop 0\n"
    "s_swappc_b64 s[34:35], s[36:37]\n"
    "s_waitcnt vmcnt(32)\n"
    "s_add_u32 s72, s32, 66560\n"
    "buffer_load_dwordx2 a[80:81], v3, s[12:15], s72 offen offset:0 nt\n"
    "buffer_load_dwordx2 a[88:89], v3, s[12:15], s72 offen offset:128 nt\n"
    "buffer_load_dwordx2 a[82:83], v3, s[12:15], s72 offen offset:256 nt\n"
    "buffer_load_dwordx2 a[90:91], v3, s[12:15], s72 offen offset:384 nt\n"
    "s_add_u32 s72, s72, 8192\n"
    "buffer_load_dwordx2 a[84:85], v3, s[12:15], s72 offen offset:0 nt\n"
    "buffer_load_dwordx2 a[92:93], v3, s[12:15], s72 offen offset:128 nt\n"
    "buffer_load_dwordx2 a[86:87], v3, s[12:15], s72 offen offset:256 nt\n"
    "buffer_load_dwordx2 a[94:95], v3, s[12:15], s72 offen offset:384 nt\n"
    "s_swappc_b64 s[34:35], s[36:37]\n"
    "s_waitcnt vmcnt(32)\n"
    "s_add_u32 s72, s32, 67584\n"
    "buffer_load_dwordx2 a[96:97], v3, s[12:15], s72 offen offset:0 nt\n"
    "buffer_load_dwordx2 a[104:105], v3, s[12:15], s72 offen offset:128 nt\n"
    "buffer_load_dwordx2 a[98:99], v3, s[12:15], s72 offen offset:256 nt\n"
    "buffer_load_dwordx2 a[106:107], v3, s[12:15], s72 offen offset:384 nt\n"
    "s_add_u32 s72, s72, 8192\n"
    "buffer_load_dwordx2 a[100:101], v3, s[12:15], s72 offen offset:0 nt\n"
    "buffer_load_dwordx2 a[108:109], v3, s[12:15], s72 offen offset:128 nt\n"
    "buffer_load_dwordx2 a[102:103], v3, s[12:15], s72 offen offset:256 nt\n"
    "buffer_load_dwordx2 a[110:111], v3, s[12:15], s72 offen offset:384 nt\n"
    "s_swappc_b64 s[34:35], s[36:37]\n"
    "s_waitcnt vmcnt(32)\n"
    "s_add_u32 s72, s32, 68608\n"
    "buffer_load_dwordx2 a[112:113], v3, s[12:15], s72 offen offset:0 nt\n"
    "buffer_load_dwordx2 a[120:121], v3, s[12:15], s72 offen offset:128 nt\n"
    "buffer_load_dwordx2 a[114:115], v3, s[12:15], s72 offen offset:256 nt\n"
    "buffer_load_dwordx2 a[122:123], v3, s[12:15], s72 offen offset:384 nt\n"
    "s_add_u32 s72, s72, 8192\n"
    "buffer_load_dwordx2 a[116:117], v3, s[12:15], s72 offen offset:0 nt\n"
    "buffer_load_dwordx2 a[124:125], v3, s[12:15], s72 offen offset:128 nt\n"
    "buffer_load_dwordx2 a[118:119], v3, s[12:15], s72 offen offset:256 nt\n"
    "buffer_load_dwordx2 a[126:127], v3, s[12:15], s72 offen offset:384 nt\n"
    "s_swappc_b64 s[34:35], s[36:37]\n"
    "s_waitcnt vmcnt(32)\n"
    "s_add_u32 s72, s32, 131072\n"
    "buffer_load_dwordx2 a[128:129], v3, s[12:15], s72 offen offset:0 nt\n"
    "buffer_load_dwordx2 a[136:137], v3, s[12:15], s72 offen offset:128 nt\n"
    "buffer_load_dwordx2 a[130:131], v3, s[12:15], s72 offen offset:256 nt\n"
    "buffer_load_dwordx2 a[138:139], v3, s[12:15], s72 offen offset:384 nt\n"
    "s_add_u32 s72, s72, 8192\n"
    "buffer_load_dwordx2 a[132:133], v3, s[12:15], s72 offen offset:0 nt\n"
    "buffer_load_dwordx2 a[140:141], v3, s[12:15], s72 offen offset:128 nt\n"
    "buffer_load_dwordx2 a[134:135], v3, s[12:15], s72 offen offset:256 nt\n"
    "buffer_load_dwordx2 a[142:143], v3, s[12:15], s72 offen offset:384 nt\n"
    "s_swappc_b64 s[34:35], s[36:37]\n"
    "s_waitcnt vmcnt(32)\n"
    "s_add_u32 s72, s32, 132096\n"
    "buffer_load_dwordx2 a[144:145], v3, s[12:15], s72 offen offset:0 nt\n"
    "buffer_load_dwordx2 a[152:153], v3, s[12:15], s72 offen offset:128 nt\n"
    "buffer_load_dwordx2 a[146:147], v3, s[12:15], s72 offen offset:256 nt\n"
    "buffer_load_dwordx2 a[154:155], v3, s[12:15], s72 offen offset:384 nt\n"
    "s_add_u32 s72, s72, 8192\n"
    "buffer_load_dwordx2 a[148:149], v3, s[12:15], s72 offen offset:0 nt\n"
    "buffer_load_dwordx2 a[156:157], v3, s[12:15], s72 offen offset:128 nt\n"
    "buffer_load_dwordx2 a[150:151], v3, s[12:15], s72 offen offset:256 nt\n"
    "buffer_load_dwordx2 a[158:159], v3, s[12:15], s72 offen offset:384 nt\n"
    "s_swappc_b64 s[34:35], s[36:37]\n"
    "s_waitcnt vmcnt(32)\n"
    "s_add_u32 s72, s32, 133120\n"
    "buffer_load_dwordx2 a[160:161], v3, s[12:15], s72 offen offset:0 nt\n"
    "buffer_load_dwordx2 a[168:169], v3, s[12:15], s72 offen offset:128 nt\n"
    "buffer_load_dwordx2 a[162:163], v3, s[12:15], s72 offen offset:256 nt\n"
    "buffer_load_dwordx2 a[170:171], v3, s[12:15], s72 offen offset:384 nt\n"
    "s_add_u32 s72, s72, 8192\n"
    "buffer_load_dwordx2 a[164:165], v3, s[12:15], s72 offen offset:0 nt\n"
    "buffer_load_dwordx2 a[172:173], v3, s[12:15], s72 offen offset:128 nt\n"
    "buffer_load_dwordx2 a[166:167], v3, s[12:15], s72 offen offset:256 nt\n"
    "buffer_load_dwordx2 a[174:175], v3, s[12:15], s72 offen offset:384 nt\n"
    "s_swappc_b64 s[34:35], s[36:37]\n"
    "s_waitcnt vmcnt(32)\n"
    "s_add_u32 s72, s32, 134144\n"
    "buffer_load_dwordx2 a[176:177], v3, s[12:15], s72 offen offset:0 nt\n"
    "buffer_load_dwordx2 a[184:185], v3, s[12:15], s72 offen offset:128 nt\n"
    "buffer_load_dwordx2 a[178:179], v3, s[12:15], s72 offen offset:256 nt\n"
    "buffer_load_dwordx2 a[186:187], v3, s[12:15], s72 offen offset:384 nt\n"
    "s_add_u32 s72, s72, 8192\n"
    "buffer_load_dwordx2 a[180:181], v3, s[12:15], s72 offen offset:0 nt\n"
    "buffer_load_dwordx2 a[188:189], v3, s[12:15], s72 offen offset:128 nt\n"
    "buffer_load_dwordx2 a[182:183], v3, s[12:15], s72 offen offset:256 nt\n"
    "buffer_load_dwordx2 a[190:191], v3, s[12:15], s72 offen offset:384 nt\n"
    "s_swappc_b64 s[34:35], s[36:37]\n"
    "s_waitcnt vmcnt(32)\n"
    "s_add_u32 s72, s32, 196608\n"
    "buffer_load_dwordx2 a[192:193], v3, s[12:15], s72 offen offset:0 nt\n"
    "buffer_load_dwordx2 a[200:201], v3, s[12:15], s72 offen offset:128 nt\n"
    "buffer_load_dwordx2 a[194:195], v3, s[12:15], s72 offen offset:256 nt\n"
    "buffer_load_dwordx2 a[202:203], v3, s[12:15], s72 offen offset:384 nt\n"
    "s_add_u32 s72, s72, 8192\n"
    "buffer_load_dwordx2 a[196:197], v3, s[12:15], s72 offen offset:0 nt\n"
    "buffer_load_dwordx2 a[204:205], v3, s[12:15], s72 offen offset:128 nt\n"
    "buffer_load_dwordx2 a[198:199], v3, s[12:15], s72 offen offset:256 nt\n"
    "buffer_load_dwordx2 a[206:207], v3, s[12:15], s72 offen offset:384 nt\n"
    "s_swappc_b64 s[34:35], s[36:37]\n"
    "s_waitcnt vmcnt(32)\n"
    "s_add_u32 s72, s32, 197632\n"
    "buffer_load_dwordx2 a[208:209], v3, s[12:15], s72 offen offset:0 nt\n"
    "buffer_load_dwordx2 a[216:217], v3, s[12:15], s72 offen offset:128 nt\n"
    "buffer_load_dwordx2 a[210:211], v3, s[12:15], s72 offen offset:256 nt\n"
    "buffer_load_dwordx2 a[218:219], v3, s[12:15], s72 offen offset:384 nt\n"
    "s_add_u32 s72, s72, 8192\n"
    "buffer_load_dwordx2 a[212:213], v3, s[12:15], s72 offen offset:0 nt\n"
    "buffer_load_dwordx2 a[220:221], v3, s[12:15], s72 offen offset:128 nt\n"
    "buffer_load_dwordx2 a[214:215], v3, s[12:15], s72 offen offset:256 nt\n"
    "buffer_load_dwordx2 a[222:223], v3, s[12:15], s72 offen offset:384 nt\n"
    "s_swappc_b64 s[34:35], s[36:37]\n"
    "s_waitcnt vmcnt(32)\n"
    "s_add_u32 s72, s32, 198656\n"
    "buffer_load_dwordx2 a[224:225], v3, s[12:15], s72 offen offset:0 nt\n"
    "buffer_load_dwordx2 a[232:233], v3, s[12:15], s72 offen offset:128 nt\n"
    "buffer_load_dwordx2 a[226:227], v3, s[12:15], s72 offen offset:256 nt\n"
    "buffer_load_dwordx2 a[234:235], v3, s[12:15], s72 offen offset:384 nt\n"
    "s_add_u32 s72, s72, 8192\n"
    "buffer_load_dwordx2 a[228:229], v3, s[12:15], s72 offen offset:0 nt\n"
    "buffer_load_dwordx2 a[236:237], v3, s[12:15], s72 offen offset:128 nt\n"
    "buffer_load_dwordx2 a[230:231], v3, s[12:15], s72 offen offset:256 nt\n"
    "buffer_load_dwordx2 a[238:239], v3, s[12:15], s72 offen offset:384 nt\n"
    "s_swappc_b64 s[34:35], s[36:37]\n"
    "s_waitcnt vmcnt(32)\n"
    "s_add_u32 s72, s32, 199680\n"
    "buffer_load_dwordx2 a[240:241], v3, s[12:15], s72 offen offset:0 nt\n"
    "buffer_load_dwordx2 a[248:249], v3, s[12:15], s72 offen offset:128 nt\n"
    "buffer_load_dwordx2 a[242:243], v3, s[12:15], s72 offen offset:256 nt\n"
    "buffer_load_dwordx2 a[250:251], v3, s[12:15], s72 offen offset:384 nt\n"
    "s_add_u32 s72, s72, 8192\n"
    "buffer_load_dwordx2 a[244:245], v3, s[12:15], s72 offen offset:0 nt\n"
    "buffer_load_dwordx2 a[252:253], v3, s[12:15], s72 offen offset:128 nt\n"
    "buffer_load_dwordx2 a[246:247], v3, s[12:15], s72 offen offset:256 nt\n"
    "buffer_load_dwordx2 a[254:255], v3, s[12:15], s72 offen offset:384 nt\n"
    "s_swappc_b64 s[34:35], s[36:37]\n"
    ".Lfb:\n"
    "s_waitcnt lgkmcnt(0)\n"
    "s_getpc_b64 s[68:69]\n"
    ".Lpc2420_.Lev:\n"
    "s_add_u32 s68, s68, .Lev-.Lpc2420_.Lev\n"
    "s_addc_u32 s69, s69, 0\n"
    ".Lfb28:\n"
    "v_cmp_gt_u32 vcc, s66, v28\n"
    "s_cbranch_vccz .Lfbn28\n"
    "s_ff1_i32_b64 s67, vcc\n"
    "v_readlane_b32 s67, v28, s67\n"
    "s_swappc_b64 s[74:75], s[68:69]\n"
    "s_nop 0\n"
    "v_cmp_eq_u32 vcc, s67, v28\n"
    "s_nop 1\n"
    "v_cndmask_b32 v40, v40, v8, vcc\n"
    "v_cndmask_b32_e64 v28, v28, -1, vcc\n"
    "s_branch .Lfb28\n"
    ".Lfbn28:\n"
    ".Lfb29:\n"
    "v_cmp_gt_u32 vcc, s66, v29\n"
    "s_cbranch_vccz .Lfbn29\n"
    "s_ff1_i32_b64 s67, vcc\n"
    "v_readlane_b32 s67, v29, s67\n"
    "s_swappc_b64 s[74:75], s[68:69]\n"
    "s_nop 0\n"
    "v_cmp_eq_u32 vcc, s67, v29\n"
    "s_nop 1\n"
    "v_cndmask_b32 v41, v41, v8, vcc\n"
    "v_cndmask_b32_e64 v29, v29, -1, vcc\n"
    "s_branch .Lfb29\n"
    ".Lfbn29:\n"
    ".Lfb30:\n"
    "v_cmp_gt_u32 vcc, s66, v30\n"
    "s_cbranch_vccz .Lfbn30\n"
    "s_ff1_i32_b64 s67, vcc\n"
    "v_readlane_b32 s67, v30, s67\n"
    "s_swappc_b64 s[74:75], s[68:69]\n"
    "s_nop 0\n"
    "v_cmp_eq_u32 vcc, s67, v30\n"
    "s_nop 1\n"
    "v_cndmask_b32 v42, v42, v8, vcc\n"
    "v_cndmask_b32_e64 v30, v30, -1, vcc\n"
    "s_branch .Lfb30\n"
    ".Lfbn30:\n"
    ".Lfb31:\n"
    "v_cmp_gt_u32 vcc, s66, v31\n"
    "s_cbranch_vccz .Lfbn31\n"
    "s_ff1_i32_b64 s67, vcc\n"
    "v_readlane_b32 s67, v31, s67\n"
    "s_swappc_b64 s[74:75], s[68:69]\n"
    "s_nop 0\n"
    "v_cmp_eq_u32 vcc, s67, v31\n"
    "s_nop 1\n"
    "v_cndmask_b32 v43, v43, v8, vcc\n"
    "v_cndmask_b32_e64 v31, v31, -1, vcc\n"
    "s_branch .Lfb31\n"
    ".Lfbn31:\n"
    ".Lfb32:\n"
    "v_cmp_gt_u32 vcc, s66, v32\n"
    "s_cbranch_vccz .Lfbn32\n"
    "s_ff1_i32_b64 s67, vcc\n"
    "v_readlane_b32 s67, v32, s67\n"
    "s_swappc_b64 s[74:75], s[68:69]\n"
    "s_nop 0\n"
    "v_cmp_eq_u32 vcc, s67, v32\n"
    "s_nop 1\n"
    "v_cndmask_b32 v44, v44, v8, vcc\n"
    "v_cndmask_b32_e64 v32, v32, -1, vcc\n"
    "s_branch .Lfb32\n"
    ".Lfbn32:\n"
    ".Lfb33:\n"
    "v_cmp_gt_u32 vcc, s66, v33\n"
    "s_cbranch_vccz .Lfbn33\n"
    "s_ff1_i32_b64 s67, vcc\n"
    "v_readlane_b32 s67, v33, s67\n"
    "s_swappc_b64 s[74:75], s[68:69]\n"
    "s_nop 0\n"
    "v_cmp_eq_u32 vcc, s67, v33\n"
    "s_nop 1\n"
    "v_cndmask_b32 v45, v45, v8, vcc\n"
    "v_cndmask_b32_e64 v33, v33, -1, vcc\n"
    "s_branch .Lfb33\n"
    ".Lfbn33:\n"
    ".Lfb34:\n"
    "v_cmp_gt_u32 vcc, s66, v34\n"
    "s_cbranch_vccz .Lfbn34\n"
    "s_ff1_i32_b64 s67, vcc\n"
    "v_readlane_b32 s67, v34, s67\n"
    "s_swappc_b64 s[74:75], s[68:69]\n"
    "s_nop 0\n"
    "v_cmp_eq_u32 vcc, s67, v34\n"
    "s_nop 1\n"
    "v_cndmask_b32 v46, v46, v8, vcc\n"
    "v_cndmask_b32_e64 v34, v34, -1, vcc\n"
    "s_branch .Lfb34\n"
    ".Lfbn34:\n"
    ".Lfb35:\n"
    "v_cmp_gt_u32 vcc, s66, v35\n"
    "s_cbranch_vccz .Lfbn35\n"
    "s_ff1_i32_b64 s67, vcc\n"
    "v_readlane_b32 s67, v35, s67\n"
    "s_swappc_b64 s[74:75], s[68:69]\n"
    "s_nop 0\n"
    "v_cmp_eq_u32 vcc, s67, v35\n"
    "s_nop 1\n"
    "v_cndmask_b32 v47, v47, v8, vcc\n"
    "v_cndmask_b32_e64 v35, v35, -1, vcc\n"
    "s_branch .Lfb35\n"
    ".Lfbn35:\n"
    ".Lfb26:\n"
    "v_cmp_gt_u32 vcc, s66, v26\n"
    "s_cbranch_vccz .Lfbn26\n"
    "s_ff1_i32_b64 s67, vcc\n"
    "v_readlane_b32 s67, v26, s67\n"
    "s_swappc_b64 s[74:75], s[68:69]\n"
    "s_nop 0\n"
    "v_cmp_eq_u32 vcc, s67, v26\n"
    "s_nop 1\n"
    "v_cndmask_b32 v48, v48, v8, vcc\n"
    "v_cndmask_b32_e64 v26, v26, -1, vcc\n"
    "s_branch .Lfb26\n"
    ".Lfbn26:\n"
    ".Lfb27:\n"
    "v_cmp_gt_u32 vcc, s66, v27\n"
    "s_cbranch_vccz .Lfbn27\n"
    "s_ff1_i32_b64 s67, vcc\n"
    "v_readlane_b32 s67, v27, s67\n"
    "s_swappc_b64 s[74:75], s[68:69]\n"
    "s_nop 0\n"
    "v_cmp_eq_u32 vcc, s67, v27\n"
    "s_nop 1\n"
    "v_cndmask_b32 v49, v49, v8, vcc\n"
    "v_cndmask_b32_e64 v27, v27, -1, vcc\n"
    "s_branch .Lfb27\n"
    ".Lfbn27:\n"
    "v_pk_add_f32 v[36:37], v[16:17], v[40:41]\n"
    "v_pk_add_f32 v[38:39], v[18:19], v[42:43]\n"
    "s_cmp_eq_u32 s41, 3\n"
    "s_branch .Lfb_r\n"
    ".Lev:\n"
    "s_lshr_b32 s64, s67, 2\n"
    "s_lshl_b32 s64, s64, 16\n"
    "s_lshl_b32 s65, s67, 30\n"
    "s_and_b32 s65, s65, 0x80000000\n"
    "s_or_b32 s64, s64, s65\n"
    "v_mov_b32 v9, 0x42800000\n"
    "v_mul_f32 v9, s64, v9\n"
    "v_lshrrev_b32 v10, 1, v4\n"
    "v_mov_b32 v8, 0\n"
    "s_mov_b32 s65, 17\n"
    ".Lev_l:\n"
    "global_load_dwordx2 v[50:51], v10, s[54:55]\n"
    "global_load_dwordx2 v[52:53], v10, s[56:57]\n"
    "global_load_dwordx2 v[54:55], v10, s[58:59]\n"
    "v_add_u32 v10, 512, v10\n"
    "s_sub_u32 s65, s65, 1\n"
    "s_waitcnt vmcnt(0)\n"
    "v_lshlrev_b32 v11, 16, v50\n"
    "v_lshlrev_b32 v12, 16, v52\n"
    "v_fma_f32 v11, v9, v11, v12\n"
    "v_mov_b32 v13, 0xc0135761\n"
    "v_mul_f32 v12, v11, v11\n"
    "v_fmac_f32 v13, 0xbdd2d3e8, v12\n"
    "v_mul_f32 v13, v13, v11\n"
    "v_exp_f32 v13, v13\n"
    "v_lshlrev_b32 v12, 16, v54\n"
    "v_mul_f32 v11, v11, v12\n"
    "v_add_f32 v13, 1.0, v13\n"
    "v_rcp_f32 v13, v13\n"
    "s_nop 0\n"
    "v_fmac_f32 v8, v11, v13\n"
    "v_and_b32 v11, v60, v50\n"
    "v_and_b32 v12, v60, v52\n"
    "v_fma_f32 v11, v9, v11, v12\n"
    "v_mov_b32 v13, 0xc0135761\n"
    "v_mul_f32 v12, v11, v11\n"
    "v_fmac_f32 v13, 0xbdd2d3e8, v12\n"
    "v_mul_f32 v13, v13, v11\n"
    "v_exp_f32 v13, v13\n"
    "v_and_b32 v12, v60, v54\n"
    "v_mul_f32 v11, v11, v12\n"
    "v_add_f32 v13, 1.0, v13\n"
    "v_rcp_f32 v13, v13\n"
    "s_nop 0\n"
    "v_fmac_f32 v8, v11, v13\n"
    "v_lshlrev_b32 v11, 16, v51\n"
    "v_lshlrev_b32 v12, 16, v53\n"
    "v_fma_f32 v11, v9, v11, v12\n"
    "v_mov_b32 v13, 0xc0135761\n"
    "v_mul_f32 v12, v11, v11\n"
    "v_fmac_f32 v13, 0xbdd2d3e8, v12\n"
    "v_mul_f32 v13, v13, v11\n"
    "v_exp_f32 v13, v13\n"
    "v_lshlrev_b32 v12, 16, v55\n"
    "v_mul_f32 v11, v11, v12\n"
    "v_add_f32 v13, 1.0, v13\n"
    "v_rcp_f32 v13, v13\n"
    "s_nop 0\n"
    "v_fmac_f32 v8, v11, v13\n"
    "v_and_b32 v11, v60, v51\n"
    "v_and_b32 v12, v60, v53\n"
    "v_fma_f32 v11, v9, v11, v12\n"
    "v_mov_b32 v13, 0xc0135761\n"
    "v_mul_f32 v12, v11, v11\n"
    "v_fmac_f32 v13, 0xbdd2d3e8, v12\n"
    "v_mul_f32 v13, v13, v11\n"
    "v_exp_f32 v13, v13\n"
    "v_and_b32 v12, v60, v55\n"
    "v_mul_f32 v11, v11, v12\n"
    "v_add_f32 v13, 1.0, v13\n"
    "v_rcp_f32 v13, v13\n"
    "s_nop 0\n"
    "v_fmac_f32 v8, v11, v13\n"
    "s_cbranch_scc0 .Lev_l\n"
    "s_nop 0\n"
    "v_add_f32_dpp v8, v8, v8 quad_perm:[1,0,3,2] row_mask:0xf bank_mask:0xf\n"
    "s_nop 1\n"
    "v_add_f32_dpp v8, v8, v8 quad_perm:[2,3,0,1] row_mask:0xf bank_mask:0xf\n"
    "s_nop 1\n"
    "v_add_f32_dpp v8, v8, v8 row_half_mirror row_mask:0xf bank_mask:0xf\n"
    "s_nop 1\n"
    "v_add_f32_dpp v8, v8, v8 row_mirror row_mask:0xf bank_mask:0xf\n"
    "s_nop 1\n"
    "v_add_f32_dpp v8, v8, v8 row_bcast:15 row_mask:0xa bank_mask:0xf\n"
    "s_nop 1\n"
    "v_add_f32_dpp v8, v8, v8 row_bcast:31 row_mask:0xc bank_mask:0xf\n"
    "v_cvt_pk_bf16_f32 v8, 0, v8\n"
    "v_add_f32 v8, s43, v8\n"
    "v_cvt_pk_bf16_f32 v8, 0, v8\n"
    "s_nop 0\n"
    "v_readlane_b32 s64, v8, 63\n"
    "s_nop 1\n"
    "v_mov_b32 v8, s64\n"
    "s_setpc_b64 s[74:75]\n"
    ".Lrdy_re:\n"
    "s_sub_u32 s40, s40, 1\n"
    "s_cbranch_scc0 .Lrdy_ag\n"
    "s_mov_b32 s40, 0\n"
    "s_branch .Lrdy_ok\n"
    ".Lrdy_ag:\n"
    "buffer_load_dwordx4 a[240:243], v4, s[16:19], 0 offen offset:256 sc1\n"
    "buffer_load_dwordx4 a[244:247], v4, s[16:19], 0 offen offset:1280 sc1\n"
    "s_waitcnt vmcnt(0)\n"
    "s_branch .Lrdy_chk\n"
    ".Lq0_re:\n"
    "s_sub_u32 s40, s40, 1\n"
    "s_cbranch_scc0 .Lq0_ag\n"
    "s_mov_b32 s40, 0\n"
    "s_branch .Lq0_ok\n"
    ".Lq0_ag:\n"
    "buffer_load_dwordx4 a[240:243], v4, s[16:19], s50 offen offset:0 sc1\n"
    "buffer_load_dwordx4 a[244:247], v4, s[16:19], s50 offen offset:1024 sc1\n"
    "buffer_load_dwordx4 a[248:251], v4, s[16:19], s50 offen offset:2048 sc1\n"
    "buffer_load_dwordx4 a[252:255], v4, s[16:19], s50 offen offset:3072 sc1\n"
    "s_waitcnt vmcnt(0)\n"
    "s_branch .Lq0_chk\n"
    ".Lqp_re:\n"
    "buffer_load_dwordx4 v[10:13], v24, s[16:19], s70 offen sc1\n"
    "s_sub_u32 s40, s40, 1\n"
    "s_waitcnt vmcnt(6)\n"
    "s_cbranch_scc0 .Lqp\n"
    "s_mov_b32 s40, 0\n"
    "s_branch .Lqp_ok\n"
    ".Lhook:\n"
    "s_swappc_b64 s[36:37], s[34:35]\n"
    "s_swappc_b64 s[36:37], s[34:35]\n"
    "s_swappc_b64 s[36:37], s[34:35]\n"
    "s_branch .Lhook_r\n"
    ".Lpvw11:\n"
    "s_waitcnt vmcnt(32)\n"
    "s_branch .Lpvr11\n"
    ".Lpvw12:\n"
    "s_waitcnt vmcnt(24)\n"
    "s_branch .Lpvr12\n"
    ".Lpvw13:\n"
    "s_waitcnt vmcnt(16)\n"
    "s_branch .Lpvr13\n"
    ".Lpvw14:\n"
    "s_waitcnt vmcnt(8)\n"
    "s_branch .Lpvr14\n"
    ".Lpvw15:\n"
    "s_waitcnt vmcnt(0)\n"
    "s_branch .Lpvr15\n"
    ".Ltask2:\n"
    "s_mov_b32 s66, 1\n"
    "s_sub_u32 s67, s31, 9\n"
    "s_cmp_eq_u32 s41, 2\n"
    "s_cbranch_scc1 .Ltask\n"
    "s_cmp_eq_u32 s41, 0\n"
    "s_cbranch_scc1 .Lpre0\n"
    "s_add_u32 s67, s67, 7\n"
    "s_cmp_lt_u32 s67, 9\n"
    "s_cbranch_scc1 .Ltask\n"
    "s_branch .Lnotask\n"
    ".Lpp_re:\n"
    "v_cmp_eq_u32 s[84:85], -1, v34\n"
    "v_cmp_eq_u32 s[86:87], -1, v35\n"
    "v_cmp_eq_u32 s[88:89], -1, v36\n"
    "v_cmp_eq_u32 s[90:91], -1, v37\n"
    "v_cmp_eq_u32 s[92:93], -1, v38\n"
    "s_mov_b64 exec, s[84:85]\n"
    "buffer_load_dwordx4 v[16:19], v11, s[16:19], s75 offen sc1\n"
    "s_mov_b64 exec, s[86:87]\n"
    "buffer_load_dwordx4 v[20:23], v11, s[16:19], s76 offen sc1\n"
    "s_mov_b64 exec, s[88:89]\n"
    "buffer_load_dwordx4 v[24:27], v11, s[16:19], s77 offen sc1\n"
    "s_mov_b64 exec, s[90:91]\n"
    "buffer_load_dwordx4 v[28:31], v11, s[16:19], s78 offen sc1\n"
    "s_mov_b64 exec, s[92:93]\n"
    "buffer_load_dwordx2 v[32:33], v10, s[16:19], s79 offen offset:1024 sc1\n"
    "s_mov_b64 exec, -1\n"
    "s_sub_u32 s40, s40, 1\n"
    "s_waitcnt vmcnt(5)\n"
    "s_cbranch_scc0 .Lpp\n"
    "s_mov_b32 s40, 0\n"
    "s_branch .Lpp_ok\n"
    ".Lfin:\n"
    "s_and_b32 s65, s29, 127\n"
    "s_cmp_lg_u32 s65, 120\n"
    "s_cbranch_scc1 .Lend\n"
    "s_lshr_b32 s65, s29, 7\n"
    "s_sub_u32 s51, 1, s65\n"
    "s_mov_b32 s64, s51\n"
    "s_branch .Lh2\n"
    ".Lyp_re:\n"
    "s_waitcnt lgkmcnt(0)\n"
    "s_sub_u32 s40, s40, 1\n"
    "s_cbranch_scc1 .Lyp_gu\n"
    "buffer_load_dwordx4 v[48:51], v9, s[16:19], s82 offen offset:2048 sc1\n"
    "buffer_load_dwordx4 v[16:19], v8, s[16:19], s74 offen sc1\n"
    "buffer_load_dwordx4 v[20:23], v8, s[16:19], s75 offen sc1\n"
    "buffer_load_dwordx4 v[24:27], v8, s[16:19], s76 offen sc1\n"
    "buffer_load_dwordx4 v[28:31], v8, s[16:19], s77 offen sc1\n"
    "buffer_load_dwordx4 v[32:35], v8, s[16:19], s78 offen sc1\n"
    "buffer_load_dwordx4 v[36:39], v8, s[16:19], s79 offen sc1\n"
    "buffer_load_dwordx4 v[40:43], v8, s[16:19], s80 offen sc1\n"
    "buffer_load_dwordx4 v[44:47], v8, s[16:19], s81 offen sc1\n"
    "s_branch .Lyp\n"
    ".Lyp_gu:\n"
    "s_mov_b32 s40, 0\n"
    "s_branch .Lyp_ok\n"
    ".Lp2:\n"
    "s_swappc_b64 s[36:37], s[34:35]\n"
    "s_branch .Lp2_r\n"
    ".Lp3:\n"
    "s_branch .Lsp0\n"
    ".Ltg_re:\n"
    "s_sub_u32 s40, s40, 1\n"
    "s_cbranch_scc0 .Ltg_ag\n"
    "s_mov_b32 s40, 0\n"
    "s_branch .Ltg_ok\n"
    ".Ltg_ag:\n"
    "s_add_u32 s64, s73, 1445888\n"
    "buffer_load_dwordx4 v[44:47], v8, s[16:19], s64 offen offset:0 sc1\n"
    "buffer_load_dwordx4 v[48:51], v8, s[16:19], s64 offen offset:16 sc1\n"
    "buffer_load_dwordx4 v[52:55], v8, s[16:19], s64 offen offset:32 sc1\n"
    "s_waitcnt vmcnt(0)\n"
    "s_branch .Ltabg\n"
    ".Lend:\n"
    : : "s"(__builtin_amdgcn_kernarg_segment_ptr()), "s"(blockIdx.x), "v"(tid)
    : "s8", "s9", "s10", "s11", "s12", "s13", "s14", "s15", "s16", "s17", "s18", "s19", "s20", "s21", "s22", "s23", "s24", "s25", "s26", "s27", "s28", "s29", "s30", "s31", "s32", "s33", "s34", "s35", "s36", "s37", "s38", "s39", "s40", "s41", "s42", "s43", "s44", "s45", "s46", "s47", "s48", "s49", "s50", "s51", "s52", "s53", "s54", "s55", "s56", "s57", "s58", "s59", "s60", "s61", "s62", "s63", "s64", "s65", "s66", "s67", "s68", "s69", "s70", "s71", "s72", "s73", "s74", "s75", "s76", "s77", "s78", "s79", "s80", "s81", "s82", "s83", "s84", "s85", "s86", "s87", "s88", "s89", "s90", "s91", "s92", "s93", "s94", "s95", "s96", "s97", "s98", "s99", "s100", "s101", "v1", "v2", "v3", "v4", "v5", "v6", "v7", "v8", "v9", "v10", "v11", "v12", "v13", "v14", "v15", "v16", "v17", "v18", "v19", "v20", "v21", "v22", "v23", "v24", "v25", "v26", "v27", "v28", "v29", "v30", "v31", "v32", "v33", "v34", "v35", "v36", "v37", "v38", "v39", "v40", "v41", "v42", "v43", "v44", "v45", "v46", "v47", "v48", "v49", "v50", "v51", "v52", "v53", "v54", "v55", "v56", "v57", "v58", "v59", "v60", "v61", "v62", "v63", "v64", "v65", "v66", "v67", "v68", "v69", "v70", "v71", "v72", "v73", "v74", "v75", "v76", "v77", "v78", "v79", "v80", "v81", "v82", "v83", "v84", "v85", "v86", "v87", "v88", "v89", "v90", "v91", "v92", "v93", "v94", "v95", "v96", "v97", "v98", "v99", "v100", "v101", "v102", "v103", "v104", "v105", "v106", "v107", "v108", "v109", "v110", "v111", "v112", "v113", "v114", "v115", "v116", "v117", "v118", "v119", "v120", "v121", "v122", "v123", "v124", "v125", "v126", "v127", "v128", "v129", "v130", "v131", "v132", "v133", "v134", "v135", "v136", "v137", "v138", "v139", "v140", "v141", "v142", "v143", "v144", "v145", "v146", "v147", "v148", "v149", "v150", "v151", "v152", "v153", "v154", "v155", "v156", "v157", "v158", "v159", "v160", "v161", "v162", "v163", "v164", "v165", "v166", "v167", "v168", "v169", "v170", "v171", "v172", "v173", "v174", "v175", "v176", "v177", "v178", "v179", "v180", "v181", "v182", "v183", "v184", "v185", "v186", "v187", "v188", "v189", "v190", "v191", "v192", "v193", "v194", "v195", "v196", "v197", "v198", "v199", "v200", "v201", "v202", "v203", "v204", "v205", "v206", "v207", "v208", "v209", "v210", "v211", "v212", "v213", "v214", "v215", "v216", "v217", "v218", "v219", "v220", "v221", "v222", "v223", "v224", "v225", "v226", "v227", "v228", "v229", "v230", "v231", "v232", "v233", "v234", "v235", "v236", "v237", "v238", "v239", "v240", "v241", "v242", "v243", "v244", "v245", "v246", "v247", "v248", "v249", "v250", "v251", "v252", "v253", "v254", "v255", "a0", "a1", "a2", "a3", "a4", "a5", "a6", "a7", "a8", "a9", "a10", "a11", "a12", "a13", "a14", "a15", "a16", "a17", "a18", "a19", "a20", "a21", "a22", "a23", "a24", "a25", "a26", "a27", "a28", "a29", "a30", "a31", "a32", "a33", "a34", "a35", "a36", "a37", "a38", "a39", "a40", "a41", "a42", "a43", "a44", "a45", "a46", "a47", "a48", "a49", "a50", "a51", "a52", "a53", "a54", "a55", "a56", "a57", "a58", "a59", "a60", "a61", "a62", "a63", "a64", "a65", "a66", "a67", "a68", "a69", "a70", "a71", "a72", "a73", "a74", "a75", "a76", "a77", "a78", "a79", "a80", "a81", "a82", "a83", "a84", "a85", "a86", "a87", "a88", "a89", "a90", "a91", "a92", "a93", "a94", "a95", "a96", "a97", "a98", "a99", "a100", "a101", "a102", "a103", "a104", "a105", "a106", "a107", "a108", "a109", "a110", "a111", "a112", "a113", "a114", "a115", "a116", "a117", "a118", "a119", "a120", "a121", "a122", "a123", "a124", "a125", "a126", "a127", "a128", "a129", "a130", "a131", "a132", "a133", "a134", "a135", "a136", "a137", "a138", "a139", "a140", "a141", "a142", "a143", "a144", "a145", "a146", "a147", "a148", "a149", "a150", "a151", "a152", "a153", "a154", "a155", "a156", "a157", "a158", "a159", "a160", "a161", "a162", "a163", "a164", "a165", "a166", "a167", "a168", "a169", "a170", "a171", "a172", "a173", "a174", "a175", "a176", "a177", "a178", "a179", "a180", "a181", "a182", "a183", "a184", "a185", "a186", "a187", "a188", "a189", "a190", "a191", "a192", "a193", "a194", "a195", "a196", "a197", "a198", "a199", "a200", "a201", "a202", "a203", "a204", "a205", "a206", "a207", "a208", "a209", "a210", "a211", "a212", "a213", "a214", "a215", "a216", "a217", "a218", "a219", "a220", "a221", "a222", "a223", "a224", "a225", "a226", "a227", "a228", "a229", "a230", "a231", "a232", "a233", "a234", "a235", "a236", "a237", "a238", "a239", "a240", "a241", "a242", "a243", "a244", "a245", "a246", "a247", "a248", "a249", "a250", "a251", "a252", "a253", "a254", "a255", "vcc", "scc", "memory");
}

extern "C" void kernel_launch(void* const* d_in, const int* in_sizes, int n_in, void* d_out, int out_size, void* d_ws, size_t ws_size, hipStream_t stream) {
  (void)hipFuncSetAttribute((const void*)fused_decode, hipFuncAttributeMaxDynamicSharedMemorySize, 163840);
  fused_decode<<<256, 256, 163840, stream>>>(d_in[0], d_in[1], d_in[2], d_in[3], d_in[4], d_in[6], d_in[7], d_in[8], d_in[9], d_out, d_ws);
}
```
